```python
import torch

def _softmax(x, axis=-1):
    return torch.softmax(x, dim=axis)
def _gelu(x):
    return torch.nn.functional.gelu(x, approximate="tanh")
def _silu(x):
    return torch.nn.functional.silu(x)
def _sigmoid(x):
    return torch.sigmoid(x)
def _layernorm(x, axis=-1, eps=1e-5):
    m = x.mean(dim=axis, keepdim=True)
    v = x.var(dim=axis, keepdim=True, unbiased=False)
    return (x - m) / torch.sqrt(v + eps)
def _scatter_add(tgt, idx, src, axis=0):
    return tgt.clone().index_add_(axis, idx, src)
def _segment_sum(src, seg, n):
    out = torch.zeros((n,) + src.shape[1:], dtype=src.dtype)
    return out.index_add_(0, seg, src)
def _pad(x, pw):
    flat = tuple(v for pair in reversed(pw) for v in pair)
    return torch.nn.functional.pad(x, flat)

kernel_name = 'recurrent_scan__linear_attn_block__mixer_s_mlp__prompt'

def setup_inputs():

    _w = (lambda a: (lambda t: t.to(torch.bfloat16) if t.is_floating_point() else t)(torch.as_tensor(a)))
    def _A_setup():
        B, S, D = 1, 2048, 640
        return {
            "x": _w((lambda *s: torch.randn(*s, dtype=torch.bfloat16) * 0.05)(B, S, D)),
            "wqkv": _w((lambda *s: torch.randn(*s, dtype=torch.bfloat16) * 0.05)(D, 3 * D)),
            "wo": _w((lambda *s: torch.randn(*s, dtype=torch.bfloat16) * 0.05)(D, D)),
        }
    _A_in = _A_setup()

    def _B_setup():
        B, S, D, F = 1, 2048, 640, 1536
        return {
            "x":  _w((lambda *s: torch.randn(*s, dtype=torch.bfloat16) * 0.05)(B, S, D)),
            "w1": _w((lambda *s: torch.randn(*s, dtype=torch.bfloat16) * 3.2)(D, D) * (D ** -0.5)),
            "w2": _w((lambda *s: torch.randn(*s, dtype=torch.bfloat16) * 3.2)(D, D) * (D ** -0.5)),
            "w3": _w((lambda *s: torch.randn(*s, dtype=torch.bfloat16) * 3.2)(D, F) * (D ** -0.5)),
            "w4": _w((lambda *s: torch.randn(*s, dtype=torch.bfloat16) * 1.6)(F, D) * (F ** -0.5)),
        }
    _B_in = _B_setup()

    return {
        "A_x": _A_in["x"],
        "A_wqkv": _A_in["wqkv"],
        "A_wo": _A_in["wo"],
        "B_x": _B_in["x"],
        "B_w1": _B_in["w1"],
        "B_w2": _B_in["w2"],
        "B_w3": _B_in["w3"],
        "B_w4": _B_in["w4"],
    }

def reference(A_x, A_wqkv, A_wo, B_x, B_w1, B_w2, B_w3, B_w4):
    def _A_ref(x, wqkv, wo):
        B, S, D = x.shape
        H = 32; Dh = D // H
        qkv = (lambda x, *d: x.permute(*d))((x @ wqkv).reshape(B, S, 3, H, Dh), 2, 0, 3, 1, 4)
        q, k, v = qkv[0], qkv[1], qkv[2]
        phi = lambda t: torch.where(t > 0, t + 1, torch.exp(t)) + 1e-6
        pq, pk = phi(q), phi(k)
        kv = torch.einsum("bhsd,bhse->bhde", pk, v)
        z = torch.einsum("bhsd->bhd", pk)
        num = torch.einsum("bhqd,bhde->bhqe", pq, kv)
        den = torch.einsum("bhqd,bhd->bhq", pq, z)[..., None] + 1e-6
        o = num / den
        return (lambda x, *d: x.permute(*d))(o, 0, 2, 1, 3).reshape(B, S, D) @ wo

    def _B_ref(x, w1, w2, w3, w4):
        B, S, D = x.shape
        h = _gelu(_layernorm(x, axis=-1) @ w1)
        c = D // 4
        g0 = _pad(h[:, :, 0*c:1*c], ((0, 0), (1, 0), (0, 0)))[:, :S, :]
        g1 = _pad(h[:, :, 1*c:2*c], ((0, 0), (0, 1), (0, 0)))[:, 1:, :]
        g2 = _pad(h[:, :, 2*c:3*c], ((0, 0), (2, 0), (0, 0)))[:, :S, :]
        g3 = _pad(h[:, :, 3*c:   ], ((0, 0), (0, 2), (0, 0)))[:, 2:, :]
        hs = torch.concatenate([g0, g1, g2, g3], axis=-1) @ w2
        x1 = x + hs
        h2 = _gelu(_layernorm(x1, axis=-1) @ w3) @ w4
        return x1 + h2

    x = A_x
    xn = _layernorm(x, axis=-1)
    s = _A_ref(xn, A_wqkv, A_wo)
    g = _silu(_B_ref(xn, B_w1, B_w2, B_w3, B_w4))
    return x + s * g
```

```cpp
#include <hip/hip_runtime.h>
#include <stdint.h>

#define LAS __attribute__((address_space(3)))
#define GAS __attribute__((address_space(1)))
#define DEVI __device__ __forceinline__

typedef __bf16 bf16x2 __attribute__((ext_vector_type(2)));
typedef __bf16 bf16x8 __attribute__((ext_vector_type(8)));
typedef float f32x2 __attribute__((ext_vector_type(2)));
typedef float f32x4 __attribute__((ext_vector_type(4)));
typedef short s16x4 __attribute__((ext_vector_type(4)));
typedef short s16x8 __attribute__((ext_vector_type(8)));
typedef unsigned u32x2 __attribute__((ext_vector_type(2)));
typedef unsigned u32x4 __attribute__((ext_vector_type(4)));

constexpr int S_ = 2048, D_ = 640, F_ = 1536;

constexpr size_t WS_HPAD = 0;
constexpr size_t WS_PQ   = WS_HPAD + (size_t)2052 * 640 * 2;
constexpr size_t WS_KVP  = WS_PQ + (size_t)2048 * 640 * 2;
constexpr size_t WS_XST  = WS_KVP + (size_t)16 * 32 * 420 * 4;
constexpr size_t WS_X1   = WS_XST + (size_t)2048 * 8;
constexpr size_t WS_X1P  = WS_X1 + (size_t)2048 * 640 * 2;
constexpr size_t WS_O    = WS_X1P + (size_t)20 * 2048 * 8;
constexpr size_t WS_HID  = WS_O + (size_t)2048 * 640 * 2;
constexpr size_t WS_S    = WS_HID + (size_t)2048 * 1536 * 2;

DEVI unsigned pack2(float a, float b) { f32x2 v = {a, b}; return __builtin_bit_cast(unsigned, __builtin_convertvector(v, bf16x2)); }
DEVI float bflo(unsigned u) { return __builtin_bit_cast(float, u << 16); }
DEVI float bfhi(unsigned u) { return __builtin_bit_cast(float, u & 0xffff0000u); }
DEVI float dot2acc(unsigned a, unsigned b, float c) { return __builtin_amdgcn_fdot2_f32_bf16(__builtin_bit_cast(bf16x2, a), __builtin_bit_cast(bf16x2, b), c, false); }
DEVI float gelu_f(float u) {
  const float c1 = -2.3022081986f, c2 = -0.10294324f;
  float w = u * (c1 + c2 * u * u);
  return u * __builtin_amdgcn_rcpf(1.0f + __builtin_amdgcn_exp2f(w));
}
DEVI float silu_f(float b) { return b * __builtin_amdgcn_rcpf(1.0f + __builtin_amdgcn_exp2f(-1.4426950409f * b)); }
DEVI float phi_f(float t) { float e = __builtin_amdgcn_exp2f(1.4426950409f * t); return (t > 0.f ? t + 1.0f : e) + 1e-6f; }
DEVI float red_g(float v) { v += __shfl_xor(v, 16); v += __shfl_xor(v, 32); return v; }
DEVI f32x4 mfma(bf16x8 a, bf16x8 b, f32x4 c) { return __builtin_amdgcn_mfma_f32_16x16x32_bf16(a, b, c, 0, 0, 0); }
DEVI bf16x8 ld_frag(LAS const char* p) { return *(LAS const bf16x8*)p; }
DEVI bf16x8 ld_tr(LAS const char* p0, LAS const char* p1) {
  s16x4 a = __builtin_amdgcn_ds_read_tr16_b64_v4i16((LAS s16x4*)p0);
  s16x4 b = __builtin_amdgcn_ds_read_tr16_b64_v4i16((LAS s16x4*)p1);
  return __builtin_bit_cast(bf16x8, __builtin_shufflevector(a, b, 0, 1, 2, 3, 4, 5, 6, 7));
}
DEVI void glds16(const char* g, LAS char* l) { __builtin_amdgcn_global_load_lds((GAS const void*)g, (LAS void*)l, 16, 0, 0); }
#define BAR() __builtin_amdgcn_s_barrier()
#define WAITV(n) asm volatile("s_waitcnt vmcnt(%0)" ::"i"(n) : "memory")
DEVI const char* pin_s(const char* p) { asm volatile("" : "+s"(p)); return p; }

template <int NI, int MAXC>
DEVI void wait_chunks(int nch) {
  if constexpr (MAXC >= 5) if (nch >= 5) { WAITV(5 * NI); return; }
  if constexpr (MAXC >= 4) if (nch == 4) { WAITV(4 * NI); return; }
  if constexpr (MAXC >= 3) if (nch == 3) { WAITV(3 * NI); return; }
  if constexpr (MAXC >= 2) if (nch == 2) { WAITV(2 * NI); return; }
  if constexpr (MAXC >= 1) if (nch == 1) { WAITV(1 * NI); return; }
  WAITV(0);
}

template <int NS, int NC, int NI, class Issue>
DEVI void loader_loop(Issue&& issue) {
#pragma unroll
  for (int c = 0; c < NS; ++c) issue(c, c);
  WAITV((NS - 2) * NI);
  BAR();
  int slot = 0;
#pragma unroll 1
  for (int c = 0; c + 1 < NC; ++c) {
    int last = (c + NS - 1 < NC - 1) ? c + NS - 1 : NC - 1;
    int allow = last - (c + 2); if (allow < 0) allow = 0;
    wait_chunks<NI, NS - 3>(allow);
    BAR();
    if (c + NS < NC) issue(c + NS, slot);
    slot = (slot + 1 == NS) ? 0 : slot + 1;
  }
}

constexpr int K1_BM = 128, K1_BN = 160, K1_NS = 4;
constexpr int K1_AB = K1_BM * 128, K1_BB = 64 * K1_BN * 2, K1_SLOT = K1_AB + K1_BB;
constexpr int K1_TAB = K1_NS * K1_SLOT;
constexpr int K1_LDS = K1_TAB + 128 * 8 + 160 * 4;

struct K1Args { const char* x; const char* wqkv; const char* w1; char* ws; };

template <int MODE>
DEVI void k1_consumer(const K1Args& a, LAS char* lds, int wid, int lane, int panel, int ct) {
  constexpr bool KV = (MODE == 1);
  const int wr = wid >> 1, wc = wid & 1, g = lane >> 4, t = lane & 15;
  const int sw = t >> 1;
  unsigned aoff[2][2];
#pragma unroll
  for (int s = 0; s < 2; ++s)
#pragma unroll
    for (int hf = 0; hf < 2; ++hf) aoff[s][hf] = (wr * 64 + ((2 * hf + 2 * wc) & 3) * 16 + t) * 128 + (((4 * s + g) ^ sw) * 16);
  const int q = t >> 2, p = lane & 3;
  unsigned boff[3];
  if constexpr (KV) {
    const unsigned rowb = (8 * g + q) * 320 + 8 * p;
    const int odd = g & 1;
    boff[0] = K1_AB + rowb + 160 * wc + (odd ? ((wc & 1) ? -32 : 32) : 0);
    boff[1] = K1_AB + rowb + 160 * wc + (odd ? ((wc & 1) ? 32 : -32) : 0);
    boff[2] = 0;
  } else {
    const unsigned rowb = K1_AB + (8 * g + q) * 320 + 160 * wc;
    const int pp = p ^ (g & 1);
    boff[0] = rowb + pp * 16 + ((p & 1) ? 8 : 0);
    boff[1] = rowb + pp * 16 + ((p & 1) ? 0 : 8);
    boff[2] = rowb + 128 + (((p >> 1) ^ (g & 1)) * 16) + (p & 1) * 8;
  }
  const int pr0 = KV ? 0 : (wr ? 64 : 0), pr1 = KV ? 0 : (wr ? 0 : 64);

  f32x4 acc[4][5];
#pragma unroll
  for (int i = 0; i < 4; ++i)
#pragma unroll
    for (int f = 0; f < 5; ++f) acc[i][f] = f32x4{0.f, 0.f, 0.f, 0.f};
  float s1[2] = {0.f, 0.f}, s2[2] = {0.f, 0.f}, cs[3] = {0.f, 0.f, 0.f};
  const unsigned ONES = 0x3f803f80u;

  bf16x8 xa[4], wa[5], xb[4], wb[5];
  auto ldfr = [&](bf16x8(&x)[4], bf16x8(&w)[5], LAS const char* sb, int s) {
#pragma unroll
    for (int j = 0; j < 4; ++j) x[j] = ld_frag(sb + aoff[s][j >> 1] + (j & 1) * 2048);
    if constexpr (KV) {
#pragma unroll
      for (int f = 0; f < 5; ++f) { LAS const char* pb = sb + boff[f & 1] + f * 32 + s * (32 * 320); w[f] = ld_tr(pb, pb + 4 * 320); }
    } else {
      { LAS const char* pb = sb + boff[0] + pr0 + s * (32 * 320); w[0] = ld_tr(pb, pb + 4 * 320); }
      { LAS const char* pb = sb + boff[1] + pr0 + s * (32 * 320); w[1] = ld_tr(pb, pb + 4 * 320); }
      { LAS const char* pb = sb + boff[0] + pr1 + s * (32 * 320); w[2] = ld_tr(pb, pb + 4 * 320); }
      { LAS const char* pb = sb + boff[1] + pr1 + s * (32 * 320); w[3] = ld_tr(pb, pb + 4 * 320); }
      { LAS const char* pb = sb + boff[2] + s * (32 * 320); w[4] = ld_tr(pb, pb + 4 * 320); }
    }
  };
  auto mm = [&](const bf16x8(&x)[4], const bf16x8(&w)[5]) {
#pragma unroll
    for (int i = 0; i < 4; ++i)
#pragma unroll
      for (int f = 0; f < 5; ++f) acc[i][f] = KV ? mfma(x[i], w[f], acc[i][f]) : mfma(w[f], x[i], acc[i][f]);
#pragma unroll
    for (int j = 0; j < 2; ++j) {
      u32x4 u = __builtin_bit_cast(u32x4, x[j]);
#pragma unroll
      for (int e = 0; e < 4; ++e) { s1[j] = dot2acc(u[e], ONES, s1[j]); s2[j] = dot2acc(u[e], u[e], s2[j]); }
    }
#pragma unroll
    for (int j = 0; j < 3; ++j) {
      u32x4 u;
      if constexpr (KV) u = __builtin_bit_cast(u32x4, wr ? w[j + 2] : w[j]);
      else u = __builtin_bit_cast(u32x4, w[j == 2 ? 4 : j]);
#pragma unroll
      for (int e = 0; e < 4; ++e) cs[j] = dot2acc(u[e], ONES, cs[j]);
    }
  };

  BAR();
  ldfr(xa, wa, lds, 0);
  int slot = 0;
#pragma unroll 1
  for (int c = 0; c < 10; ++c) {
    LAS const char* sb = lds + slot * K1_SLOT;
    slot = (slot + 1 == K1_NS) ? 0 : slot + 1;
    ldfr(xb, wb, sb, 1);
    mm(xa, wa);
    if (c < 9) ldfr(xa, wa, lds + slot * K1_SLOT, 0);
    mm(xb, wb);
    if (c < 9) BAR();
  }

  LAS f32x2* stat = (LAS f32x2*)(lds + K1_TAB);
  LAS float* cst = (LAS float*)(lds + K1_TAB + 128 * 8);
#pragma unroll
  for (int j = 0; j < 2; ++j) {
    float a1 = red_g(s1[j]), a2 = red_g(s2[j]);
    float m = a1 * (1.0f / 640.0f);
    float var = fmaxf(a2 * (1.0f / 640.0f) - m * m, 0.f);
    float r = __builtin_amdgcn_rsqf(var + 1e-5f);
    int row = wr * 64 + ((j + 2 * wc) & 3) * 16 + t;
    if (g == 0) stat[row] = f32x2{m, r};
  }
#pragma unroll
  for (int j = 0; j < 3; ++j) {
    float c = red_g(cs[j]);
    int col;
    if constexpr (KV) col = 80 * wc + 16 * (j + 2 * wr) + t;
    else {
      if (j == 2) col = 80 * wc + 64 + t;
      else col = 80 * wc + (wr ? 32 : 0) + 8 * (t >> 2) + 4 * ((j ^ (t >> 2)) & 1) + (t & 3);
    }
    if (g == 0) cst[col] = c;
  }
  BAR();

  if constexpr (KV) {
    f32x2 st[4][4];
#pragma unroll
    for (int j = 0; j < 4; ++j) {
      int row = wr * 64 + ((j + 2 * wc) & 3) * 16 + 4 * g;
#pragma unroll
      for (int r = 0; r < 4; ++r) st[j][r] = stat[row + r];
    }
    bf16x8 op[2][5];
#pragma unroll
    for (int f = 0; f < 5; ++f) {
      float c = cst[80 * wc + 16 * f + t];
      bool isk = (f < 2) || (f == 2 && t < 8);
#pragma unroll
      for (int h = 0; h < 2; ++h) {
        u32x4 u;
#pragma unroll
        for (int jj = 0; jj < 2; ++jj) {
          float v[4];
#pragma unroll
          for (int r = 0; r < 4; ++r) {
            float y = st[2 * h + jj][r].y * (acc[2 * h + jj][f][r] - st[2 * h + jj][r].x * c);
            if (f <= 2) { float ph = phi_f(y); y = (f < 2 || isk) ? ph : y; }
            v[r] = y;
          }
          u[2 * jj] = pack2(v[0], v[1]); u[2 * jj + 1] = pack2(v[2], v[3]);
        }
        op[h][f] = __builtin_bit_cast(bf16x8, u);
      }
    }
    const bf16x8 ones = __builtin_bit_cast(bf16x8, u32x4{ONES, ONES, ONES, ONES});
    f32x4 d[10];
    const int pa[7] = {0, 0, 1, 1, 1, 2, 2}, pb[7] = {2, 3, 2, 3, 4, 3, 4};
#pragma unroll
    for (int k = 0; k < 7; ++k) { d[k] = mfma(op[0][pa[k]], op[0][pb[k]], f32x4{0.f, 0.f, 0.f, 0.f}); d[k] = mfma(op[1][pa[k]], op[1][pb[k]], d[k]); }
#pragma unroll
    for (int k = 0; k < 3; ++k) { d[7 + k] = mfma(op[0][k], ones, f32x4{0.f, 0.f, 0.f, 0.f}); d[7 + k] = mfma(op[1][k], ones, d[7 + k]); }
    LAS f32x4* red = (LAS f32x4*)lds;
#pragma unroll
    for (int k = 0; k < 10; ++k) red[(wid * 10 + k) * 64 + lane] = d[k];
    BAR();
    LAS const float* rf = (LAS const float*)lds;
    float* kvp = (float*)(a.ws + WS_KVP) + ((size_t)panel * 32 + (ct - 4) * 4) * 420;
    for (int idx = wid * 64 + lane; idx < 1680; idx += 256) {
      int hl = idx / 420, rem = idx - hl * 420, w2 = hl >> 1, hb = hl & 1;
      int dd, k, col;
      if (rem < 400) {
        dd = rem / 20; int e = rem - dd * 20;
        int kc = 20 * hb + dd, vc = 40 + 20 * hb + e;
        int fa = kc >> 4, fb = vc >> 4;
        k = (fa == 0) ? fb - 2 : (fa == 1) ? fb : fb + 2;
        col = vc & 15; dd = kc & 15;
      } else {
        int kc = 20 * hb + (rem - 400);
        k = 7 + (kc >> 4); col = 0; dd = kc & 15;
      }
      int el = (k * 64 + (dd >> 2) * 16 + col) * 4 + (dd & 3);
      kvp[idx] = rf[(w2)*2560 + el] + rf[(2 + w2) * 2560 + el];
    }
  } else {
    constexpr bool isq = (MODE == 0);
    f32x2 st[4];
#pragma unroll
    for (int j = 0; j < 4; ++j) st[j] = stat[wr * 64 + ((j + 2 * wc) & 3) * 16 + t];
    if (ct == 12 && wc == 0 && g == 0) {
      f32x2* xs = (f32x2*)(a.ws + WS_XST);
#pragma unroll
      for (int j = 0; j < 4; ++j) xs[panel * 128 + wr * 64 + j * 16 + t] = st[j];
    }
    f32x4 c4[5];
    {
      const int hE = 4 * (g & 1), hO = 4 - hE;
      c4[0] = *(LAS const f32x4*)(cst + 80 * wc + pr0 / 2 + 8 * g + hE);
      c4[1] = *(LAS const f32x4*)(cst + 80 * wc + pr0 / 2 + 8 * g + hO);
      c4[2] = *(LAS const f32x4*)(cst + 80 * wc + pr1 / 2 + 8 * g + hE);
      c4[3] = *(LAS const f32x4*)(cst + 80 * wc + pr1 / 2 + 8 * g + hO);
      c4[4] = *(LAS const f32x4*)(cst + 80 * wc + 64 + 4 * g);
    }
    char* obase; unsigned rstride;
    if (isq) { obase = a.ws + WS_PQ + (size_t)(2 * ct + wc) * (2048 * 160); rstride = 160; }
    else { obase = a.ws + WS_HPAD + 2 * 1280 + (size_t)(ct - 12) * 320 + wc * 160; rstride = 1280; }
#pragma unroll
    for (int j = 0; j < 4; ++j) {
      const int tok = panel * 128 + wr * 64 + ((j + 2 * wc) & 3) * 16 + t;
      float m = st[j].x, r = st[j].y;
      if (!isq) { float v1 = r * r; float var = fmaxf(1.0f - 1e-5f * v1, 0.f); r *= __builtin_amdgcn_rsqf(var + 1e-5f); }
      unsigned pk[5][2];
#pragma unroll
      for (int f = 0; f < 5; ++f) {
        float v[4];
#pragma unroll
        for (int e = 0; e < 4; ++e) {
          float y = r * (acc[j][f][e] - m * c4[f][e]);
          v[e] = isq ? phi_f(y) : gelu_f(y);
        }
        pk[f][0] = pack2(v[0], v[1]); pk[f][1] = pack2(v[2], v[3]);
      }
      char* orow = obase + (size_t)tok * rstride;
      const bool od = g & 1;
      *(u32x4*)(orow + pr0 + 16 * g) = od ? u32x4{pk[1][0], pk[1][1], pk[0][0], pk[0][1]} : u32x4{pk[0][0], pk[0][1], pk[1][0], pk[1][1]};
      *(u32x4*)(orow + pr1 + 16 * g) = od ? u32x4{pk[3][0], pk[3][1], pk[2][0], pk[2][1]} : u32x4{pk[2][0], pk[2][1], pk[3][0], pk[3][1]};
      *(u32x2*)(orow + 128 + 8 * g) = u32x2{pk[4][0], pk[4][1]};
    }
    if (!isq && (panel == 0 || panel == 15) && wid == 0 && lane < 40) {
      char* z = a.ws + WS_HPAD + (size_t)(panel == 0 ? 0 : 2050) * 1280 + (ct - 12) * 320;
      *(u32x4*)(z + (lane / 20) * 1280 + (lane % 20) * 16) = u32x4{0, 0, 0, 0};
    }
  }
}

__global__ __launch_bounds__(512) void k1_kernel(K1Args a) {
  extern __shared__ __attribute__((aligned(16))) char smem[];
  LAS char* lds = (LAS char*)smem;
  const int wid = __builtin_amdgcn_readfirstlane(threadIdx.x >> 6), lane = threadIdx.x & 63;
  const int xcd = blockIdx.x & 7, j = blockIdx.x >> 3;
  const int panel = (xcd & 1) * 8 + (j & 7), ct = (xcd >> 1) * 4 + (j >> 3);
  const bool kv = (ct >= 4 && ct < 12);
  if (wid >= 4) {
    const int w = wid - 4;
    unsigned voffA[4], voffB[5];
#pragma unroll
    for (int i = 0; i < 4; ++i) {
      int row = (w + 4 * i) * 8 + (lane >> 3), pc = (lane & 7) ^ ((row >> 1) & 7);
      voffA[i] = (panel * 128 + row) * 1280 + pc * 16;
    }
    const char* bB; unsigned ldb;
    if (ct < 4) { bB = a.wqkv + ct * 320; ldb = 3840; }
    else if (ct < 12) { bB = a.wqkv + 1280 + (ct - 4) * 160; ldb = 3840; }
    else { bB = a.w1 + (ct - 12) * 320; ldb = 1280; }
#pragma unroll
    for (int i = 0; i < 5; ++i) {
      int P = (w + 4 * i) * 64 + lane, k = P / 20, qq = P - k * 20;
      unsigned cb;
      if (kv) { int sp = qq ^ (((k >> 3) & 1) << 1); int blk = sp / 5, r5 = sp - blk * 5; cb = (blk & 1) * 1280 + (blk >> 1) * 80 + r5 * 16; }
      else { int sp = qq ^ ((k >> 3) & 1); cb = sp * 16; }
      voffB[i] = k * ldb + cb;
    }
    const char* bA = a.x;
    auto issue = [&](int c, int slot) {
      const char* pa = pin_s(bA + c * 128);
      const char* pb = pin_s(bB + (size_t)c * 64 * ldb);
      LAS char* sb = lds + slot * K1_SLOT + w * 1024;
#pragma unroll
      for (int i = 0; i < 4; ++i) glds16(pa + voffA[i], sb + i * 4096);
#pragma unroll
      for (int i = 0; i < 5; ++i) glds16(pb + voffB[i], sb + K1_AB + i * 4096);
    };
    loader_loop<K1_NS, 10, 9>(issue);
    return;
  }
  if (kv) k1_consumer<1>(a, lds, wid, lane, panel, ct);
  else if (ct < 4) k1_consumer<0>(a, lds, wid, lane, panel, ct);
  else k1_consumer<2>(a, lds, wid, lane, panel, ct);
}

constexpr int T_NS = 7, T_AB = 96 * 128, T_BB = 64 * 128, T_SLOT = T_AB + T_BB;
constexpr int T_LDS = T_NS * T_SLOT;

template <int NC>
DEVI void t96_consumer(LAS char* lds, int wid, int lane, f32x4 (&acc)[3][2]) {
  const int wr = wid >> 1, wc = wid & 1, g = lane >> 4, t = lane & 15, sw = t >> 1;
  unsigned aoff[2];
#pragma unroll
  for (int s = 0; s < 2; ++s) aoff[s] = (wr * 48 + t) * 128 + (((4 * s + g) ^ sw) * 16);
  const int q = t >> 2, p = lane & 3;
  const int swz = (((q >> 1) & 1) << 2) | (g & 1);
  const unsigned rowb = T_AB + (8 * g + q) * 128 + (((4 * wc + p) ^ swz) * 16);
  const unsigned bE = rowb + ((p & 1) ? 8 : 0), bO = rowb + ((p & 1) ? 0 : 8);
#pragma unroll
  for (int i = 0; i < 3; ++i) { acc[i][0] = f32x4{0.f, 0.f, 0.f, 0.f}; acc[i][1] = acc[i][0]; }
  bf16x8 xa[3], wa[2], xb[3], wb[2];
  auto ldfr = [&](bf16x8(&x)[3], bf16x8(&w)[2], LAS const char* sb, int s) {
#pragma unroll
    for (int j = 0; j < 3; ++j) x[j] = ld_frag(sb + aoff[s] + j * 2048);
    w[0] = ld_tr(sb + bE + s * 4096, sb + bE + s * 4096 + 512);
    w[1] = ld_tr(sb + bO + s * 4096, sb + bO + s * 4096 + 512);
  };
  auto mm = [&](const bf16x8(&x)[3], const bf16x8(&w)[2]) {
#pragma unroll
    for (int i = 0; i < 3; ++i) { acc[i][0] = mfma(w[0], x[i], acc[i][0]); acc[i][1] = mfma(w[1], x[i], acc[i][1]); }
  };
  BAR();
  ldfr(xa, wa, lds, 0);
  int slot = 0;
#pragma unroll 1
  for (int c = 0; c < NC; ++c) {
    LAS const char* sb = lds + slot * T_SLOT;
    slot = (slot + 1 == T_NS) ? 0 : slot + 1;
    ldfr(xb, wb, sb, 1);
    mm(xa, wa);
    if (c < NC - 1) ldfr(xa, wa, lds + slot * T_SLOT, 0);
    mm(xb, wb);
    if (c < NC - 1) BAR();
  }
}

template <int NC, bool SHIFT>
DEVI void t96_loader(LAS char* lds, int w, int lane, const char* A, unsigned lda, int row0, const char* B, unsigned ldb) {
  unsigned voffA[3], voffB[2];
  const int pslot = lane & 7;
  int pcs[3];
#pragma unroll
  for (int i = 0; i < 3; ++i) {
    int row = (w + 4 * i) * 8 + (lane >> 3), pc = pslot ^ ((row >> 1) & 7);
    int gr = row0 + row; if (gr > S_ - 1) gr = S_ - 1;
    voffA[i] = gr * lda + pc * 16; pcs[i] = pc;
  }
#pragma unroll
  for (int i = 0; i < 2; ++i) {
    int k = (w + 4 * i) * 8 + (lane >> 3);
    int sp = pslot ^ ((((k >> 1) & 1) << 2) | ((k >> 3) & 1));
    voffB[i] = k * ldb + sp * 16;
  }
  auto issue = [&](int c, int slot) {
    const char* pa = pin_s(A + c * 128);
    const char* pb = pin_s(B + (size_t)c * 64 * ldb);
    LAS char* sb = lds + slot * T_SLOT + w * 1024;
#pragma unroll
    for (int i = 0; i < 3; ++i) {
      unsigned vo = voffA[i];
      if constexpr (SHIFT) {
        int k = c * 64 + pcs[i] * 8;
        int sh = (k < 160) ? -1 : (k < 320) ? 1 : (k < 480) ? -2 : 2;
        vo += (2 + sh) * 1280;
      }
      glds16(pa + vo, sb + i * 4096);
    }
#pragma unroll
    for (int i = 0; i < 2; ++i) glds16(pb + voffB[i], sb + T_AB + i * 4096);
  };
  loader_loop<T_NS, NC, 5>(issue);
}

DEVI void pair8(const f32x4& e, const f32x4& o, int g, float (&v)[8]) {
  const bool od = g & 1;
#pragma unroll
  for (int r = 0; r < 4; ++r) { v[r] = od ? o[r] : e[r]; v[4 + r] = od ? e[r] : o[r]; }
}

struct K2Args { const char* x; const char* w2; char* ws; };

DEVI void k2_side(const K2Args& a, LAS char* lds, int wid, int lane, int hg, int tq) {
  const int tid = wid * 64 + lane;
  const char* src = a.ws + WS_PQ + ((size_t)hg * 2048 + tq * 512) * 160;
#pragma unroll
  for (int i = 0; i < 10; ++i) glds16(src + (size_t)((wid * 10 + i) * 1024 + lane * 16), lds + (wid * 10 + i) * 1024);
  LAS float* kvs = (LAS float*)(lds + 81920);
  if (tid < 420) {
    const f32x4* p = (const f32x4*)(a.ws + WS_KVP) + (size_t)hg * 420 + tid;
    f32x4 s = p[0];
#pragma unroll
    for (int i = 1; i < 16; ++i) s += p[(size_t)i * 32 * 105];
    *(LAS f32x4*)(kvs + tid * 4) = s;
  }
  WAITV(0);
  __syncthreads();
  const int g = lane >> 4, t = lane & 15;
#pragma unroll 1
  for (int j = 0; j < 4; ++j) {
    LAS const float* kh = kvs + j * 420;
    u32x4 u0, u1, uz;
#pragma unroll
    for (int e = 0; e < 4; ++e) {
      int d0 = 8 * g + 2 * e, d1 = d0 + 1;
      bool v = d0 < 20;
      int dd0 = v ? d0 : 0, dd1 = v ? d1 : 0;
      float a0 = kh[dd0 * 20 + t], a1 = kh[dd1 * 20 + t];
      float b0 = kh[dd0 * 20 + 16 + (t & 3)], b1 = kh[dd1 * 20 + 16 + (t & 3)];
      float z0 = kh[400 + dd0], z1 = kh[400 + dd1];
      u0[e] = v ? pack2(a0, a1) : 0u;
      u1[e] = (v && t < 4) ? pack2(b0, b1) : 0u;
      uz[e] = v ? pack2(z0, z1) : 0u;
    }
    const bf16x8 A0 = __builtin_bit_cast(bf16x8, u0), A1 = __builtin_bit_cast(bf16x8, u1), AZ = __builtin_bit_cast(bf16x8, uz);
#pragma unroll
    for (int i = 0; i < 4; ++i) {
      LAS char* row = lds + (wid * 64 + i * 16 + t) * 160 + 40 * j;
      u32x2 lo = *(LAS const u32x2*)(row + 16 * (g < 3 ? g : 0)), hi = *(LAS const u32x2*)(row + 16 * (g < 2 ? g : 0) + 8);
      u32x4 ub = {g < 3 ? lo[0] : 0u, g < 3 ? lo[1] : 0u, g < 2 ? hi[0] : 0u, g < 2 ? hi[1] : 0u};
      const bf16x8 Bq = __builtin_bit_cast(bf16x8, ub);
      const f32x4 z4 = {0.f, 0.f, 0.f, 0.f};
      f32x4 d0 = mfma(A0, Bq, z4), d1 = mfma(A1, Bq, z4), dz = mfma(AZ, Bq, z4);
      float inv = 1.0f / (dz[0] + 1e-6f);
      *(LAS u32x2*)(row + 8 * g) = u32x2{pack2(d0[0] * inv, d0[1] * inv), pack2(d0[2] * inv, d0[3] * inv)};
      if (g == 0) *(LAS u32x2*)(row + 32) = u32x2{pack2(d1[0] * inv, d1[1] * inv), pack2(d1[2] * inv, d1[3] * inv)};
    }
  }
  char* dst = a.ws + WS_O + (size_t)(tq * 512 + wid * 64) * 1280 + hg * 160;
#pragma unroll
  for (int i = 0; i < 10; ++i) {
    int P = i * 64 + lane, r = P / 10, qq = P - r * 10;
    u32x4 v = *(LAS const u32x4*)(lds + (wid * 64) * 160 + P * 16);
    *(u32x4*)(dst + (size_t)r * 1280 + qq * 16) = v;
  }
}

__global__ __launch_bounds__(512) void k2_kernel(K2Args a) {
  extern __shared__ __attribute__((aligned(16))) char smem[];
  LAS char* lds = (LAS char*)smem;
  const int wid = __builtin_amdgcn_readfirstlane(threadIdx.x >> 6), lane = threadIdx.x & 63;
  const int xcd = blockIdx.x & 7, j = blockIdx.x >> 3;
  if (j >= 28) { int sidx = (j - 28) * 8 + xcd; k2_side(a, lds, wid, lane, sidx & 7, sidx >> 3); return; }
  const int tile = xcd * 28 + j;
  if (tile >= 220) return;
  const int panel = tile / 10, ct = tile - panel * 10;
  if (wid >= 4) {
    t96_loader<10, true>(lds, wid - 4, lane, a.ws + WS_HPAD, 1280, panel * 96, a.w2 + ct * 128, 1280);
    return;
  }
  const int wr = wid >> 1, wc = wid & 1, g = lane >> 4, t = lane & 15;
  u32x4 xv[3]; f32x2 st[3]; int tok[3];
#pragma unroll
  for (int i = 0; i < 3; ++i) {
    tok[i] = panel * 96 + wr * 48 + i * 16 + t;
    int tk = tok[i] < S_ ? tok[i] : S_ - 1;
    xv[i] = *(const u32x4*)(a.x + (size_t)tk * 1280 + ct * 128 + wc * 64 + g * 16);
    st[i] = ((const f32x2*)(a.ws + WS_XST))[tk];
  }
  f32x4 acc[3][2];
  t96_consumer<10>(lds, wid, lane, acc);
#pragma unroll
  for (int i = 0; i < 3; ++i) {
    float v[8];
    pair8(acc[i][0], acc[i][1], g, v);
    u32x4 o; float s1 = 0.f, s2 = 0.f;
#pragma unroll
    for (int e = 0; e < 4; ++e) {
      unsigned xn = pack2((bflo(xv[i][e]) - st[i].x) * st[i].y, (bfhi(xv[i][e]) - st[i].x) * st[i].y);
      o[e] = pack2(bflo(xn) + v[2 * e], bfhi(xn) + v[2 * e + 1]);
      s1 = dot2acc(o[e], 0x3f803f80u, s1); s2 = dot2acc(o[e], o[e], s2);
    }
    s1 = red_g(s1); s2 = red_g(s2);
    if (tok[i] < S_) {
      *(u32x4*)(a.ws + WS_X1 + (size_t)tok[i] * 1280 + ct * 128 + wc * 64 + g * 16) = o;
      if (g == 0) ((f32x2*)(a.ws + WS_X1P))[(size_t)(2 * ct + wc) * 2048 + tok[i]] = f32x2{s1, s2};
    }
  }
}

constexpr int K3_NS = 4, K3_AB = 144 * 128, K3_BB = 64 * 256, K3_SLOT = K3_AB + K3_BB;
constexpr int K3_TAB = K3_NS * K3_SLOT;
constexpr int K3_LDS = K3_TAB + 144 * 8 + 128 * 4;

struct K3Args { const char* w3; const char* wo; char* ws; };

template <bool MLP>
DEVI void k3_consumer(const K3Args& a, LAS char* lds, int wid, int lane, int panel, int ct) {
  const int g = lane >> 4, t = lane & 15, sw = t >> 1;
  LAS f32x2* stat = (LAS f32x2*)(lds + K3_TAB);
  LAS float* cst = (LAS float*)(lds + K3_TAB + 144 * 8);
  if constexpr (MLP) {
    int tid = wid * 64 + lane;
    if (tid < 144) {
      int tk = panel * 144 + tid; if (tk > S_ - 1) tk = S_ - 1;
      const f32x2* p = (const f32x2*)(a.ws + WS_X1P) + tk;
      f32x2 s = p[0];
#pragma unroll
      for (int i = 1; i < 20; ++i) s += p[(size_t)i * 2048];
      float m = s.x * (1.0f / 640.0f);
      float var = fmaxf(s.y * (1.0f / 640.0f) - m * m, 0.f);
      stat[tid] = f32x2{m, __builtin_amdgcn_rsqf(var + 1e-5f)};
    }
    asm volatile("s_waitcnt lgkmcnt(0)" ::: "memory");
  }
  unsigned aoff[2];
#pragma unroll
  for (int s = 0; s < 2; ++s) aoff[s] = t * 128 + (((4 * s + g) ^ sw) * 16);
  const int q = t >> 2, p = lane & 3;
  const int swz = (q << 2) | (g & 1);
  const unsigned rowb = K3_AB + (8 * g + q) * 256 + (((4 * wid + p) ^ swz) * 16);
  const unsigned bE = rowb + ((p & 1) ? 8 : 0), bO = rowb + ((p & 1) ? 0 : 8);
  f32x4 acc[9][2];
#pragma unroll
  for (int i = 0; i < 9; ++i) { acc[i][0] = f32x4{0.f, 0.f, 0.f, 0.f}; acc[i][1] = acc[i][0]; }
  float cs[2] = {0.f, 0.f};
  bf16x8 xa[9], wa[2], xb[9], wb[2];
  auto ldfr = [&](bf16x8(&x)[9], bf16x8(&w)[2], LAS const char* sb, int s) {
    w[0] = ld_tr(sb + bE + s * 8192, sb + bE + s * 8192 + 1024);
    w[1] = ld_tr(sb + bO + s * 8192, sb + bO + s * 8192 + 1024);
#pragma unroll
    for (int j = 0; j < 9; ++j) x[j] = ld_frag(sb + aoff[s] + j * 2048);
  };
  auto mm = [&](const bf16x8(&x)[9], const bf16x8(&w)[2]) {
#pragma unroll
    for (int i = 0; i < 9; ++i) { acc[i][0] = mfma(w[0], x[i], acc[i][0]); acc[i][1] = mfma(w[1], x[i], acc[i][1]); }
    if constexpr (MLP) {
#pragma unroll
      for (int f = 0; f < 2; ++f) {
        u32x4 u = __builtin_bit_cast(u32x4, w[f]);
#pragma unroll
        for (int e = 0; e < 4; ++e) cs[f] = dot2acc(u[e], 0x3f803f80u, cs[f]);
      }
    }
  };
  BAR();
  ldfr(xa, wa, lds, 0);
  int slot = 0;
#pragma unroll 1
  for (int c = 0; c < 10; ++c) {
    LAS const char* sb = lds + slot * K3_SLOT;
    slot = (slot + 1 == K3_NS) ? 0 : slot + 1;
    ldfr(xb, wb, sb, 1);
    mm(xa, wa);
    if (c < 9) ldfr(xa, wa, lds + slot * K3_SLOT, 0);
    mm(xb, wb);
    if (c < 9) BAR();
  }
  f32x4 cE = {0.f, 0.f, 0.f, 0.f}, cO = cE;
  if constexpr (MLP) {
#pragma unroll
    for (int f = 0; f < 2; ++f) {
      float c = red_g(cs[f]);
      if (g == 0) cst[wid * 32 + f * 16 + t] = c;
    }
    asm volatile("s_waitcnt lgkmcnt(0)" ::: "memory");
    cE = *(LAS const f32x4*)(cst + wid * 32 + 4 * g);
    cO = *(LAS const f32x4*)(cst + wid * 32 + 16 + 4 * g);
  }
  char* obase = MLP ? a.ws + WS_HID + ct * 256 + wid * 64 + g * 16 : a.ws + WS_S + (ct - 12) * 256 + wid * 64 + g * 16;
  const unsigned rs = MLP ? 3072 : 1280;
  const bool od = g & 1;
#pragma unroll
  for (int i = 0; i < 9; ++i) {
    const int tok = panel * 144 + i * 16 + t;
    float e4[4], o4[4];
    if constexpr (MLP) {
      f32x2 st = stat[i * 16 + t];
#pragma unroll
      for (int r = 0; r < 4; ++r) { e4[r] = gelu_f(st.y * (acc[i][0][r] - st.x * cE[r])); o4[r] = gelu_f(st.y * (acc[i][1][r] - st.x * cO[r])); }
    } else {
#pragma unroll
      for (int r = 0; r < 4; ++r) { e4[r] = acc[i][0][r]; o4[r] = acc[i][1][r]; }
    }
    unsigned e0 = pack2(e4[0], e4[1]), e1 = pack2(e4[2], e4[3]), o0 = pack2(o4[0], o4[1]), o1 = pack2(o4[2], o4[3]);
    if (tok < S_) *(u32x4*)(obase + (size_t)tok * rs) = od ? u32x4{o0, o1, e0, e1} : u32x4{e0, e1, o0, o1};
  }
}

__global__ __launch_bounds__(512) void k3_kernel(K3Args a) {
  extern __shared__ __attribute__((aligned(16))) char smem[];
  LAS char* lds = (LAS char*)smem;
  const int wid = __builtin_amdgcn_readfirstlane(threadIdx.x >> 6), lane = threadIdx.x & 63;
  const int xcd = blockIdx.x & 7, j = blockIdx.x >> 3;
  const int tile = xcd * 32 + j;
  if (tile >= 255) return;
  const int panel = tile / 17, ct = tile - panel * 17;
  const bool mlp = ct < 12;
  if (wid >= 4) {
    const int w = wid - 4;
    const char* A = mlp ? a.ws + WS_X1 : a.ws + WS_O;
    const char* B = mlp ? a.w3 + ct * 256 : a.wo + (ct - 12) * 256;
    const unsigned ldb = mlp ? 3072 : 1280;
    unsigned voffA[5], voffB[4];
#pragma unroll
    for (int i = 0; i < 5; ++i) {
      int ii = w + 4 * i; if (ii > 17) ii = 17;
      int row = ii * 8 + (lane >> 3), pc = (lane & 7) ^ ((row >> 1) & 7);
      int gr = panel * 144 + row; if (gr > S_ - 1) gr = S_ - 1;
      voffA[i] = gr * 1280 + pc * 16;
    }
#pragma unroll
    for (int i = 0; i < 4; ++i) {
      int P = (w + 4 * i) * 64 + lane, k = P >> 4, qq = P & 15;
      int sp = qq ^ (((k & 3) << 2) | ((k >> 3) & 1));
      voffB[i] = k * ldb + sp * 16;
    }
    const int last = (w + 16 > 17) ? 17 : w + 16;
    auto issue = [&](int c, int slot) {
      const char* pa = pin_s(A + c * 128);
      const char* pb = pin_s(B + (size_t)c * 64 * ldb);
      LAS char* sb = lds + slot * K3_SLOT;
#pragma unroll
      for (int i = 0; i < 4; ++i) glds16(pa + voffA[i], sb + (w + 4 * i) * 1024);
      glds16(pa + voffA[4], sb + last * 1024);
#pragma unroll
      for (int i = 0; i < 4; ++i) glds16(pb + voffB[i], sb + K3_AB + (w + 4 * i) * 1024);
    };
    loader_loop<K3_NS, 10, 9>(issue);
    return;
  }
  if (mlp) k3_consumer<true>(a, lds, wid, lane, panel, ct);
  else k3_consumer<false>(a, lds, wid, lane, panel, ct);
}

struct K4Args { const char* x; const char* w4; char* ws; char* out; };

__global__ __launch_bounds__(512) void k4_kernel(K4Args a) {
  extern __shared__ __attribute__((aligned(16))) char smem[];
  LAS char* lds = (LAS char*)smem;
  const int wid = __builtin_amdgcn_readfirstlane(threadIdx.x >> 6), lane = threadIdx.x & 63;
  const int xcd = blockIdx.x & 7, j = blockIdx.x >> 3;
  const int tile = xcd * 28 + j;
  if (j >= 28 || tile >= 220) return;
  const int panel = tile / 10, ct = tile - panel * 10;
  if (wid >= 4) {
    t96_loader<24, false>(lds, wid - 4, lane, a.ws + WS_HID, 3072, panel * 96, a.w4 + ct * 128, 1280);
    return;
  }
  const int wr = wid >> 1, wc = wid & 1, g = lane >> 4, t = lane & 15;
  u32x4 xv[3], x1v[3], sv[3]; int tok[3];
#pragma unroll
  for (int i = 0; i < 3; ++i) {
    tok[i] = panel * 96 + wr * 48 + i * 16 + t;
    int tk = tok[i] < S_ ? tok[i] : S_ - 1;
    size_t off = (size_t)tk * 1280 + ct * 128 + wc * 64 + g * 16;
    xv[i] = *(const u32x4*)(a.x + off);
    x1v[i] = *(const u32x4*)(a.ws + WS_X1 + off);
    sv[i] = *(const u32x4*)(a.ws + WS_S + off);
  }
  f32x4 acc[3][2];
  t96_consumer<24>(lds, wid, lane, acc);
#pragma unroll
  for (int i = 0; i < 3; ++i) {
    float v[8];
    pair8(acc[i][0], acc[i][1], g, v);
    u32x4 o;
#pragma unroll
    for (int e = 0; e < 4; ++e) {
      float b0 = bflo(x1v[i][e]) + v[2 * e], b1 = bfhi(x1v[i][e]) + v[2 * e + 1];
      o[e] = pack2(bflo(xv[i][e]) + bflo(sv[i][e]) * silu_f(b0), bfhi(xv[i][e]) + bfhi(sv[i][e]) * silu_f(b1));
    }
    if (tok[i] < S_) *(u32x4*)(a.out + (size_t)tok[i] * 1280 + ct * 128 + wc * 64 + g * 16) = o;
  }
}

extern "C" void kernel_launch(void* const* d_in, const int* in_sizes, int n_in, void* d_out, int out_size, void* d_ws, size_t ws_size,
                              hipStream_t stream) {
  const char* x = (const char*)d_in[0];
  const char* wqkv = (const char*)d_in[1];
  const char* wo = (const char*)d_in[2];
  const char* w1 = (const char*)d_in[4];
  const char* w2 = (const char*)d_in[5];
  const char* w3 = (const char*)d_in[6];
  const char* w4 = (const char*)d_in[7];
  char* ws = (char*)d_ws;
  hipFuncSetAttribute((const void*)k1_kernel, hipFuncAttributeMaxDynamicSharedMemorySize, K1_LDS);
  hipFuncSetAttribute((const void*)k2_kernel, hipFuncAttributeMaxDynamicSharedMemorySize, T_LDS);
  hipFuncSetAttribute((const void*)k3_kernel, hipFuncAttributeMaxDynamicSharedMemorySize, K3_LDS);
  hipFuncSetAttribute((const void*)k4_kernel, hipFuncAttributeMaxDynamicSharedMemorySize, T_LDS);
  k1_kernel<<<256, 512, K1_LDS, stream>>>(K1Args{x, wqkv, w1, ws});
  k2_kernel<<<256, 512, T_LDS, stream>>>(K2Args{x, w2, ws});
  k3_kernel<<<256, 512, K3_LDS, stream>>>(K3Args{w3, wo, ws});
  k4_kernel<<<256, 512, T_LDS, stream>>>(K4Args{x, w4, ws, (char*)d_out});
}
```

```cpp
#include <hip/hip_runtime.h>
#include <stdint.h>

#define LAS __attribute__((address_space(3)))
#define GAS __attribute__((address_space(1)))
#define DEVI __device__ __forceinline__

typedef __bf16 bf16x2 __attribute__((ext_vector_type(2)));
typedef __bf16 bf16x8 __attribute__((ext_vector_type(8)));
typedef float f32x2 __attribute__((ext_vector_type(2)));
typedef float f32x4 __attribute__((ext_vector_type(4)));
typedef short s16x4 __attribute__((ext_vector_type(4)));
typedef unsigned u32x2 __attribute__((ext_vector_type(2)));
typedef unsigned u32x4 __attribute__((ext_vector_type(4)));
typedef __amdgpu_buffer_rsrc_t rsrc_t;

#define ST_AUX 16
#define OUT_AUX 0
#define USE_ROT 0
#define USE_PF 1

#define T_R 4
#define ROT1 0
#define PF1 1
constexpr int S_ = 2048;

#define LD_A 1408
#define LD_HID 3200
constexpr unsigned WS_HPAD = 0;
constexpr unsigned WS_PQ   = WS_HPAD + 2052 * LD_A;
constexpr unsigned WS_KVP  = WS_PQ + 2048 * 640 * 2;
constexpr unsigned WS_XST  = WS_KVP + 16 * 32 * 420 * 4;
constexpr unsigned WS_X1   = WS_XST + 2048 * 8;
constexpr unsigned WS_X1P  = WS_X1 + 2048 * LD_A;
constexpr unsigned WS_O    = WS_X1P + 20 * 2048 * 8;
constexpr unsigned WS_HID  = WS_O + 2048 * LD_A;
constexpr unsigned WS_S    = WS_HID + 2048 * LD_HID;
constexpr unsigned WS_END  = WS_S + 2048 * 640 * 2;

template <int V> struct IC { static constexpr int value = V; };
DEVI unsigned pack2(float a, float b) { f32x2 v = {a, b}; return __builtin_bit_cast(unsigned, __builtin_convertvector(v, bf16x2)); }
DEVI unsigned pack2v(f32x2 v) { return __builtin_bit_cast(unsigned, __builtin_convertvector(v, bf16x2)); }
DEVI float bflo(unsigned u) { return __builtin_bit_cast(float, u << 16); }
DEVI float bfhi(unsigned u) { return __builtin_bit_cast(float, u & 0xffff0000u); }
DEVI f32x2 unpack2(unsigned u) { return f32x2{bflo(u), bfhi(u)}; }
DEVI float dot2acc(unsigned a, unsigned b, float c) { return __builtin_amdgcn_fdot2_f32_bf16(__builtin_bit_cast(bf16x2, a), __builtin_bit_cast(bf16x2, b), c, false); }
DEVI f32x2 lo2(f32x4 v) { return f32x2{v[0], v[1]}; }
DEVI f32x2 hi2(f32x4 v) { return f32x2{v[2], v[3]}; }
DEVI f32x2 gelu2(f32x2 u) {
  const float c1 = -2.3022081986f, c2 = -0.10294324f;
  f32x2 w = u * (u * u * c2 + c1);
  f32x2 d = f32x2{__builtin_amdgcn_exp2f(w[0]), __builtin_amdgcn_exp2f(w[1])} + 1.0f;
  return u * f32x2{__builtin_amdgcn_rcpf(d[0]), __builtin_amdgcn_rcpf(d[1])};
}
DEVI f32x2 silu2(f32x2 b) {
  f32x2 w = b * -1.4426950409f;
  f32x2 d = f32x2{__builtin_amdgcn_exp2f(w[0]), __builtin_amdgcn_exp2f(w[1])} + 1.0f;
  return b * f32x2{__builtin_amdgcn_rcpf(d[0]), __builtin_amdgcn_rcpf(d[1])};
}
DEVI f32x2 phi2(f32x2 t) {
  f32x2 w = t * 1.4426950409f;
  f32x2 e = {__builtin_amdgcn_exp2f(fminf(w[0], 0.f)), __builtin_amdgcn_exp2f(fminf(w[1], 0.f))};
  return (f32x2{fmaxf(t[0], 0.f), fmaxf(t[1], 0.f)} + e) + 1e-6f;
}
DEVI float red_g(float v) { v += __shfl_xor(v, 16); v += __shfl_xor(v, 32); return v; }
DEVI f32x4 mfma(bf16x8 a, bf16x8 b, f32x4 c) { return __builtin_amdgcn_mfma_f32_16x16x32_bf16(a, b, c, 0, 0, 0); }
DEVI bf16x8 ld_frag(LAS const char* p) { return *(LAS const bf16x8*)p; }
DEVI bf16x8 ld_tr(LAS const char* p0, LAS const char* p1) {
  s16x4 a = __builtin_amdgcn_ds_read_tr16_b64_v4i16((LAS s16x4*)p0);
  s16x4 b = __builtin_amdgcn_ds_read_tr16_b64_v4i16((LAS s16x4*)p1);
  return __builtin_bit_cast(bf16x8, __builtin_shufflevector(a, b, 0, 1, 2, 3, 4, 5, 6, 7));
}
DEVI void glds16(const char* g, LAS char* l) { __builtin_amdgcn_global_load_lds((GAS const void*)g, (LAS void*)l, 16, 0, 0); }
DEVI rsrc_t mk_rsrc(const void* p, unsigned bytes) { return __builtin_amdgcn_make_buffer_rsrc((void*)p, 0, (int)bytes, 0x00020000); }
template <int AUX> DEVI void st16(rsrc_t r, unsigned off, u32x4 v) { __builtin_amdgcn_raw_buffer_store_b128(v, r, off, 0, AUX); }
template <int AUX> DEVI void st8(rsrc_t r, unsigned off, u32x2 v) { __builtin_amdgcn_raw_buffer_store_b64(v, r, off, 0, AUX); }
template <int AUX> DEVI void st8f(rsrc_t r, unsigned off, f32x2 v) { __builtin_amdgcn_raw_buffer_store_b64(__builtin_bit_cast(u32x2, v), r, off, 0, AUX); }
template <int AUX> DEVI void st4f(rsrc_t r, unsigned off, float v) { __builtin_amdgcn_raw_buffer_store_b32(__builtin_bit_cast(unsigned, v), r, off, 0, AUX); }
DEVI u32x4 ld16(rsrc_t r, unsigned off) { return __builtin_amdgcn_raw_buffer_load_b128(r, off, 0, 0); }
DEVI f32x2 ld8f(rsrc_t r, unsigned off) { return __builtin_bit_cast(f32x2, __builtin_amdgcn_raw_buffer_load_b64(r, off, 0, 0)); }
#define CBAR() asm volatile("" ::: "memory")
#define BAR() do { CBAR(); __builtin_amdgcn_s_barrier(); CBAR(); } while (0)
#define SB0() __builtin_amdgcn_sched_barrier(0)
#define LGKM0() __builtin_amdgcn_s_waitcnt(0xC07F)
#define WAITV(n) asm volatile("s_waitcnt vmcnt(%0)" ::"i"(n) : "memory")
DEVI const char* pin_s(const char* p) { asm volatile("" : "+s"(p)); return p; }
template <class T> DEVI void pin_v(T& v) { asm volatile("" : "+v"(v)); }

#define TS(ws, k, j) do {} while (0)

template <int NS, int NC, int NI, int R, int LAG, int XI, class Issue, class Hook>
DEVI void loader_loop(Issue&& issue_, Hook&& hook) {
  auto& issue = issue_;
#pragma unroll
  for (int k = 0; k < R; ++k) issue(k, k);
  WAITV((R - 1) * NI);
  BAR();
#pragma unroll
  for (int k = R; k < NS; ++k) issue(k, k);
#pragma unroll
  for (int k = 1; k < LAG; ++k) { WAITV((NS - 1 - k) * NI); BAR(); }
  int slot = 0;
#pragma unroll 1
  for (int k = LAG; k <= NC - NS + LAG - 1; ++k) {
    WAITV((NS - LAG - 1) * NI);
    BAR();
    issue(k - LAG + NS, slot);
    slot = (slot + 1 == NS) ? 0 : slot + 1;
  }
  hook();
#pragma unroll
  for (int k = NC - NS + LAG; k < NC; ++k) {
    WAITV((NC - k - 1) * NI + XI);
    BAR();
  }
}

template <int NM, int ND, int NV>
DEVI void sched_region() {
  constexpr int DPER = (ND + NM - 3) / (NM - 2);
  constexpr int VPER = (NV + NM - 1) / NM;
#pragma unroll
  for (int i = 0; i < NM; ++i) {
    __builtin_amdgcn_sched_group_barrier(0x008, 1, 0);
    if (i * DPER < ND) {
      __builtin_amdgcn_sched_group_barrier(0x002, 1, 0);
      __builtin_amdgcn_sched_group_barrier(0x100, DPER, 0);
    }
    if (VPER > 0) __builtin_amdgcn_sched_group_barrier(0x002, VPER, 0);
    __builtin_amdgcn_sched_group_barrier(0x004, 1, 0);
  }
}

template <int NC, int NS, int SLOT, int NM, int ND, int NV, class LD, class MM, class PRE>
DEVI void consumer_loop(LAS const char* lds, LD&& ld_, MM&& mm_, PRE&& pre) {
  auto& ld = ld_; auto& mm = mm_;
  BAR();
  ld(IC<0>{}, lds, IC<0>{});
  SB0();
  pre();
  SB0();
  LAS const char* sb = lds;
#pragma unroll 1
  for (int c = 0; c < NC - 1; ++c) {
    ld(IC<1>{}, sb, IC<1>{});
    mm(IC<0>{});
    sb = (sb == lds + (NS - 1) * SLOT) ? lds : sb + SLOT;
    sched_region<NM, ND, NV>();
    SB0();
    LGKM0();
    BAR();
    SB0();
    ld(IC<0>{}, sb, IC<0>{});
    mm(IC<1>{});
    sched_region<NM, ND, NV>();
    SB0();
  }
  ld(IC<1>{}, sb, IC<1>{});
  mm(IC<0>{});
  sched_region<NM, ND, NV>();
  SB0();
  mm(IC<1>{});
}

template <int NC, int NS, int SLOT, int NM, int ND, class LD, class MM, class PRE>
DEVI void consumer_loop_full(LAS const char* lds, LD&& ld, MM&& mm, PRE&& pre) {
  BAR();
  ld(IC<0>{}, lds);
  SB0();
  pre();
  SB0();
  LAS const char* s1 = lds + SLOT;
#pragma unroll 1
  for (int c = 0; c + 2 < NC; c += 2) {
    LAS const char* s2 = (s1 == lds + (NS - 1) * SLOT) ? lds : s1 + SLOT;
    BAR();
    SB0();
    ld(IC<1>{}, s1);
    mm(IC<0>{});
    s1 = (s2 == lds + (NS - 1) * SLOT) ? lds : s2 + SLOT;
    sched_region<NM, ND, 0>();
    SB0();
    BAR();
    SB0();
    ld(IC<0>{}, s2);
    mm(IC<1>{});
    sched_region<NM, ND, 0>();
    SB0();
  }
  BAR();
  SB0();
  ld(IC<1>{}, s1);
  mm(IC<0>{});
  sched_region<NM, ND, 0>();
  SB0();
  mm(IC<1>{});
}

constexpr int K1_BM = 128, K1_BN = 160, K1_NS = 4;
constexpr int K1_AB = K1_BM * 128, K1_BB = 64 * K1_BN * 2, K1_SLOT = K1_AB + K1_BB;
constexpr int K1_TAB = K1_NS * K1_SLOT;
constexpr int K1_DUMMY = K1_TAB + 128 * 8 + 160 * 4;
constexpr int K1_LDS = K1_DUMMY + 256;

struct K1Args { const char* x; const char* wqkv; const char* w1; const char* w2; const char* w3; const char* w4; const char* wo; char* ws; };

template <int MODE>
DEVI void k1_consumer(const K1Args& a, LAS char* lds, int wid, int lane, int panel, int ct) {
  constexpr bool KV = (MODE == 1);
  const int wr = wid >> 1, wc = wid & 1, g = lane >> 4, t = lane & 15;
  const int sw = t >> 1;
  unsigned aoff[2][2];
#pragma unroll
  for (int s = 0; s < 2; ++s)
#pragma unroll
    for (int hf = 0; hf < 2; ++hf) aoff[s][hf] = (wr * 64 + ((2 * hf + 2 * wc) & 3) * 16 + t) * 128 + (((4 * s + g) ^ sw) * 16);
  const int q = t >> 2, p = lane & 3;
  unsigned boff[5];
  if constexpr (KV) {
    const unsigned rowb = (8 * g + q) * 320 + 8 * p;
    const int odd = g & 1;
    boff[0] = K1_AB + rowb + 160 * wc + (odd ? ((wc & 1) ? -32 : 32) : 0);
    boff[1] = K1_AB + rowb + 160 * wc + (odd ? ((wc & 1) ? 32 : -32) : 0);
    boff[2] = boff[3] = boff[4] = 0;
  } else {
    const unsigned rowb = K1_AB + (8 * g + q) * 320 + 160 * wc;
    const int pp = p ^ (g & 1);
    const int pr0 = wr ? 64 : 0, pr1 = wr ? 0 : 64;
    const unsigned bE = rowb + pp * 16 + ((p & 1) ? 8 : 0), bO = rowb + pp * 16 + ((p & 1) ? 0 : 8);
    boff[0] = bE + pr0; boff[1] = bO + pr0; boff[2] = bE + pr1; boff[3] = bO + pr1;
    boff[4] = rowb + 128 + (((p >> 1) ^ (g & 1)) * 16) + (p & 1) * 8;
  }
  unsigned el[7];
  if constexpr (KV) {
#pragma unroll
    for (int i = 0; i < 7; ++i) {
      int idx = wid * 64 + lane + 256 * i;
      int hl = idx / 420, rem = idx - hl * 420, w2 = hl >> 1, hb = hl & 1;
      int dd, k, col;
      if (rem < 400) {
        dd = rem / 20; int e = rem - dd * 20;
        int kc = 20 * hb + dd, vc = 40 + 20 * hb + e;
        int fa = kc >> 4, fb = vc >> 4;
        k = (fa == 0) ? fb - 2 : (fa == 1) ? fb : fb + 2;
        col = vc & 15; dd = kc & 15;
      } else {
        int kc = 20 * hb + (rem - 400);
        k = 7 + (kc >> 4); col = 0; dd = kc & 15;
      }
      el[i] = (((k * 64 + (dd >> 2) * 16 + col) * 4 + (dd & 3)) + (w2 & 1) * 2560) * 4;
      pin_v(el[i]);
    }
  }

  f32x4 acc[4][5];
#pragma unroll
  for (int i = 0; i < 4; ++i)
#pragma unroll
    for (int f = 0; f < 5; ++f) { acc[i][f] = f32x4{0.f, 0.f, 0.f, 0.f}; pin_v(acc[i][f]); }
  f32x4 s1[2], s2[2], cs[3];
#pragma unroll
  for (int j = 0; j < 2; ++j) { s1[j] = f32x4{0.f, 0.f, 0.f, 0.f}; s2[j] = s1[j]; pin_v(s1[j]); pin_v(s2[j]); }
#pragma unroll
  for (int j = 0; j < 3; ++j) { cs[j] = f32x4{0.f, 0.f, 0.f, 0.f}; pin_v(cs[j]); }
  const unsigned ONES = 0x3f803f80u;
  bf16x8 ones8 = __builtin_bit_cast(bf16x8, u32x4{ONES, ONES, ONES, ONES});
  pin_v(ones8);

  bf16x8 X[2][4], W[2][5];
  auto ld = [&](auto SET, LAS const char* sb, auto SS) {
    constexpr int st = decltype(SET)::value, s = decltype(SS)::value;
    if constexpr (KV) {
      X[st][0] = ld_frag(sb + aoff[s][0]);
      { LAS const char* pb = sb + boff[0] + s * (32 * 320); W[st][0] = ld_tr(pb, pb + 4 * 320); }
      { LAS const char* pb = sb + boff[1] + 32 + s * (32 * 320); W[st][1] = ld_tr(pb, pb + 4 * 320); }
      { LAS const char* pb = sb + boff[0] + 64 + s * (32 * 320); W[st][2] = ld_tr(pb, pb + 4 * 320); }
      { LAS const char* pb = sb + boff[1] + 96 + s * (32 * 320); W[st][3] = ld_tr(pb, pb + 4 * 320); }
      { LAS const char* pb = sb + boff[0] + 128 + s * (32 * 320); W[st][4] = ld_tr(pb, pb + 4 * 320); }
      X[st][1] = ld_frag(sb + aoff[s][0] + 2048);
      X[st][2] = ld_frag(sb + aoff[s][1]);
      X[st][3] = ld_frag(sb + aoff[s][1] + 2048);
    } else {
      X[st][0] = ld_frag(sb + aoff[s][0]);
#pragma unroll
      for (int f = 0; f < 5; ++f) { LAS const char* pb = sb + boff[f] + s * (32 * 320); W[st][f] = ld_tr(pb, pb + 4 * 320); }
      X[st][1] = ld_frag(sb + aoff[s][0] + 2048);
      X[st][2] = ld_frag(sb + aoff[s][1]);
      X[st][3] = ld_frag(sb + aoff[s][1] + 2048);
    }
  };
  auto mm = [&](auto SET) {
    constexpr int st = decltype(SET)::value;
#pragma unroll
    for (int i = 0; i < 4; ++i)
#pragma unroll
      for (int f = 0; f < 5; ++f) acc[i][f] = KV ? mfma(X[st][i], W[st][f], acc[i][f]) : mfma(W[st][f], X[st][i], acc[i][f]);
#pragma unroll
    for (int j = 0; j < 2; ++j) { s1[j] = mfma(ones8, X[st][j], s1[j]); s2[j] = mfma(X[st][j], X[st][j], s2[j]); }
#pragma unroll
    for (int j = 0; j < 3; ++j) {
      if constexpr (KV) cs[j] = mfma(ones8, wr ? W[st][j + 2] : W[st][j], cs[j]);
      else cs[j] = mfma(W[st][j == 2 ? 4 : j], ones8, cs[j]);
    }
  };
  consumer_loop<10, K1_NS, K1_SLOT, 27, 14, KV ? 12 : 0>(lds, ld, mm, [&]() { TS(a.ws, 0, 1); });
  TS(a.ws, 0, 2);

  LAS float* mean = (LAS float*)(lds + K1_TAB);
  LAS float* rstd = mean + 128;
  LAS float* cst = mean + 256;
#pragma unroll
  for (int j = 0; j < 2; ++j) {
    const int r3 = t & 3;
    float a1 = s1[j][0], a2 = r3 == 0 ? s2[j][0] : r3 == 1 ? s2[j][1] : r3 == 2 ? s2[j][2] : s2[j][3];
    float m = a1 * (1.0f / 640.0f);
    float var = fmaxf(a2 * (1.0f / 640.0f) - m * m, 0.f);
    float r = __builtin_amdgcn_rsqf(var + 1e-5f);
    int row = wr * 64 + ((j + 2 * wc) & 3) * 16 + t;
    if (g == (t >> 2)) { mean[row] = m; rstd[row] = r; }
  }
#pragma unroll
  for (int j = 0; j < 3; ++j) {
    if constexpr (KV) {
      if (g == 0) cst[80 * wc + 16 * (j + 2 * wr) + t] = cs[j][0];
    } else {
      int col = (j == 2) ? 80 * wc + 64 + 4 * g : 80 * wc + (wr ? 32 : 0) + 8 * g + 4 * ((j ^ g) & 1);
      if (t == 0) *(LAS f32x4*)(cst + col) = cs[j];
    }
  }
  LGKM0();
  BAR();

  const rsrc_t rw = mk_rsrc(a.ws, WS_END);
  if constexpr (KV) {
    f32x4 M4[4], R4[4];
#pragma unroll
    for (int j = 0; j < 4; ++j) {
      int row = wr * 64 + ((j + 2 * wc) & 3) * 16 + 4 * g;
      M4[j] = *(LAS const f32x4*)(mean + row); R4[j] = *(LAS const f32x4*)(rstd + row);
    }
    bf16x8 op[2][5];
#pragma unroll
    for (int f = 0; f < 5; ++f) {
      const float c = cst[80 * wc + 16 * f + t];
      const bool isk = (f < 2) || (f == 2 && t < 8);
#pragma unroll
      for (int h = 0; h < 2; ++h) {
        u32x4 u;
#pragma unroll
        for (int jj = 0; jj < 2; ++jj) {
          const int j = 2 * h + jj;
          f32x2 y0 = lo2(R4[j]) * (lo2(acc[j][f]) - lo2(M4[j]) * c);
          f32x2 y1 = hi2(R4[j]) * (hi2(acc[j][f]) - hi2(M4[j]) * c);
          if (f <= 2) {
            f32x2 p0 = phi2(y0), p1 = phi2(y1);
            if (f < 2) { y0 = p0; y1 = p1; } else { y0 = isk ? p0 : y0; y1 = isk ? p1 : y1; }
          }
          u[2 * jj] = pack2v(y0); u[2 * jj + 1] = pack2v(y1);
        }
        op[h][f] = __builtin_bit_cast(bf16x8, u);
      }
    }
    const bf16x8 ones = __builtin_bit_cast(bf16x8, u32x4{ONES, ONES, ONES, ONES});
    f32x4 d[10];
    constexpr int pa[7] = {0, 0, 1, 1, 1, 2, 2}, pb[7] = {2, 3, 2, 3, 4, 3, 4};
#pragma unroll
    for (int k = 0; k < 7; ++k) { d[k] = mfma(op[0][pa[k]], op[0][pb[k]], f32x4{0.f, 0.f, 0.f, 0.f}); d[k] = mfma(op[1][pa[k]], op[1][pb[k]], d[k]); }
#pragma unroll
    for (int k = 0; k < 3; ++k) { d[7 + k] = mfma(op[0][k], ones, f32x4{0.f, 0.f, 0.f, 0.f}); d[7 + k] = mfma(op[1][k], ones, d[7 + k]); }
    LAS f32x4* red = (LAS f32x4*)lds;
#pragma unroll
    for (int k = 0; k < 10; ++k) red[(wid * 10 + k) * 64 + lane] = d[k];
    LGKM0();
    BAR();
    const unsigned kvp = WS_KVP + (panel * 32 + (ct - 4) * 4) * 420 * 4 + (wid * 64 + lane) * 4;
#pragma unroll
    for (int i = 0; i < 7; ++i) {
      float v = *(LAS const float*)(lds + el[i]) + *(LAS const float*)(lds + el[i] + 2 * 2560 * 4);
      if (i < 6 || wid * 64 + lane < 1680 - 6 * 256) st4f<ST_AUX>(rw, kvp + i * 1024, v);
    }
  } else {
    constexpr bool isq = (MODE == 0);
    const int pr0 = wr ? 64 : 0, pr1 = wr ? 0 : 64;
    float m4[4], r4[4];
#pragma unroll
    for (int j = 0; j < 4; ++j) { int row = wr * 64 + ((j + 2 * wc) & 3) * 16 + t; m4[j] = mean[row]; r4[j] = rstd[row]; }
    if (ct == 12 && wc == 0 && g == 0) {
#pragma unroll
      for (int j = 0; j < 4; ++j) st8f<ST_AUX>(rw, WS_XST + (panel * 128 + wr * 64 + j * 16 + t) * 8, f32x2{m4[j], r4[j]});
    }
    f32x4 c4[5];
    {
      const int hE = 4 * (g & 1), hO = 4 - hE;
      c4[0] = *(LAS const f32x4*)(cst + 80 * wc + pr0 / 2 + 8 * g + hE);
      c4[1] = *(LAS const f32x4*)(cst + 80 * wc + pr0 / 2 + 8 * g + hO);
      c4[2] = *(LAS const f32x4*)(cst + 80 * wc + pr1 / 2 + 8 * g + hE);
      c4[3] = *(LAS const f32x4*)(cst + 80 * wc + pr1 / 2 + 8 * g + hO);
      c4[4] = *(LAS const f32x4*)(cst + 80 * wc + 64 + 4 * g);
    }
    unsigned obase, rstride;
    if (isq) { obase = WS_PQ + (2 * ct + wc) * (2048 * 160); rstride = 160; }
    else { obase = WS_HPAD + 2 * LD_A + (ct - 12) * 320 + wc * 160; rstride = LD_A; }
    const bool od = g & 1;
#pragma unroll
    for (int j = 0; j < 4; ++j) {
      const int tok = panel * 128 + wr * 64 + ((j + 2 * wc) & 3) * 16 + t;
      const float m = m4[j];
      float r = r4[j];
      if (!isq) { float var = fmaxf(1.0f - 1e-5f * r * r, 0.f); r *= __builtin_amdgcn_rsqf(var + 1e-5f); }
      unsigned pk[5][2];
#pragma unroll
      for (int f = 0; f < 5; ++f) {
        f32x2 y0 = (lo2(acc[j][f]) - lo2(c4[f]) * m) * r, y1 = (hi2(acc[j][f]) - hi2(c4[f]) * m) * r;
        pk[f][0] = pack2v(isq ? phi2(y0) : gelu2(y0)); pk[f][1] = pack2v(isq ? phi2(y1) : gelu2(y1));
      }
      const unsigned orow = obase + tok * rstride;
      st16<ST_AUX>(rw, orow + pr0 + 16 * g, od ? u32x4{pk[1][0], pk[1][1], pk[0][0], pk[0][1]} : u32x4{pk[0][0], pk[0][1], pk[1][0], pk[1][1]});
      st16<ST_AUX>(rw, orow + pr1 + 16 * g, od ? u32x4{pk[3][0], pk[3][1], pk[2][0], pk[2][1]} : u32x4{pk[2][0], pk[2][1], pk[3][0], pk[3][1]});
      st8<ST_AUX>(rw, orow + 128 + 8 * g, u32x2{pk[4][0], pk[4][1]});
    }
    if (!isq && (panel == 0 || panel == 15) && wid == 0 && lane < 40)
      st16<ST_AUX>(rw, WS_HPAD + (panel == 0 ? 0 : 2050) * LD_A + (ct - 12) * 320 + (lane / 20) * LD_A + (lane % 20) * 16, u32x4{0, 0, 0, 0});
  }
}

__global__ __launch_bounds__(512) void k1_kernel(K1Args a) {
  extern __shared__ __attribute__((aligned(16))) char smem[];
  LAS char* lds = (LAS char*)smem;
  const int wid = __builtin_amdgcn_readfirstlane(threadIdx.x >> 6), lane = threadIdx.x & 63;
  const int xcd = blockIdx.x & 7, j = blockIdx.x >> 3;
  const int panel = (xcd & 1) * 8 + (j & 7), ct = (xcd >> 1) * 4 + (j >> 3);
  const bool kv = (ct >= 4 && ct < 12);
  TS(a.ws, 0, 0);
  if (wid >= 4) {
    const int w = wid - 4;
    unsigned voffA[4], voffB[5];
#pragma unroll
    for (int i = 0; i < 4; ++i) {
      int row = (w + 4 * i) * 8 + (lane >> 3), pc = (lane & 7) ^ ((row >> 1) & 7);
      voffA[i] = (panel * 128 + row) * 1280 + pc * 16;
    }
    const char* bB; unsigned ldb;
    if (ct < 4) { bB = a.wqkv + ct * 320; ldb = 3840; }
    else if (ct < 12) { bB = a.wqkv + 1280 + (ct - 4) * 160; ldb = 3840; }
    else { bB = a.w1 + (ct - 12) * 320; ldb = 1280; }
#pragma unroll
    for (int i = 0; i < 5; ++i) {
      int P = (w + 4 * i) * 64 + lane, k = P / 20, qq = P - k * 20;
      unsigned cb;
      if (kv) { int sp = qq ^ (((k >> 3) & 1) << 1); int blk = sp / 5, r5 = sp - blk * 5; cb = (blk & 1) * 1280 + (blk >> 1) * 80 + r5 * 16; }
      else { int sp = qq ^ ((k >> 3) & 1); cb = sp * 16; }
      voffB[i] = k * ldb + cb;
    }
    const int rot = ROT1 ? (((j & 7) * 5) / 4 + ((j >> 3) * 5) / 2) % 10 : 0;
    const char* bA = a.x;
    auto issue = [&](int k, int slot) {
      int c = k + rot; if (c >= 10) c -= 10;
      const char* pa = pin_s(bA + c * 128);
      const char* pb = pin_s(bB + (size_t)c * 64 * ldb);
      LAS char* sb = lds + slot * K1_SLOT + w * 1024;
#pragma unroll
      for (int i = 0; i < 4; ++i) glds16(pa + voffA[i], sb + i * 4096);
#pragma unroll
      for (int i = 0; i < 5; ++i) glds16(pb + voffB[i], sb + K1_AB + i * 4096);
    };
    loader_loop<K1_NS, 10, 9, 2, 1, USE_PF ? 1 : 0>(issue, [&]() {
      int id = blockIdx.x * 256 + w * 64 + lane;
      if (id >= 43520) id -= 43520;
      const char* p;
      if (id < 6400) p = a.w2 + (size_t)id * 128;
      else if (id < 12800) p = a.wo + (size_t)(id - 6400) * 128;
      else if (id < 28160) p = a.w3 + (size_t)(id - 12800) * 128;
      else p = a.w4 + (size_t)(id - 28160) * 128;
      __builtin_amdgcn_global_load_lds((GAS const void*)p, (LAS void*)(lds + K1_DUMMY), 4, 0, 0);
    });
    return;
  }
  {
    int id = blockIdx.x * 256 + wid * 64 + lane;
    if (id >= 46080) id -= 46080;
    const char* p;
    if (id < 20480) p = a.x + (size_t)id * 128;
    else if (id < 39680) p = a.wqkv + (size_t)(id - 20480) * 128;
    else p = a.w1 + (size_t)(id - 39680) * 128;
    __builtin_amdgcn_global_load_lds((GAS const void*)p, (LAS void*)(lds + K1_DUMMY), 4, 0, 0);
  }
  if (kv) k1_consumer<1>(a, lds, wid, lane, panel, ct);
  else if (ct < 4) k1_consumer<0>(a, lds, wid, lane, panel, ct);
  else k1_consumer<2>(a, lds, wid, lane, panel, ct);
  TS(a.ws, 0, 3);
}

constexpr int T_NS = 7, T_AB = 96 * 128, T_BB = 64 * 128, T_SLOT = T_AB + T_BB;
constexpr int T_LDS = T_NS * T_SLOT;

template <int NC>
DEVI void t96_consumer(LAS char* lds, int wid, int lane, f32x4 (&acc)[3][2], char* ws, int kk) {
  const int wr = wid >> 1, wc = wid & 1, g = lane >> 4, t = lane & 15, sw = t >> 1;
  unsigned aoff[2];
#pragma unroll
  for (int s = 0; s < 2; ++s) aoff[s] = (wr * 48 + t) * 128 + (((4 * s + g) ^ sw) * 16);
  const int q = t >> 2, p = lane & 3;
  const int swz = (((q >> 1) & 1) << 2) | (g & 1);
  const unsigned rowb = T_AB + (8 * g + q) * 128 + (((4 * wc + p) ^ swz) * 16);
  const unsigned bE = rowb + ((p & 1) ? 8 : 0), bO = rowb + ((p & 1) ? 0 : 8);
#pragma unroll
  for (int i = 0; i < 3; ++i) { acc[i][0] = f32x4{0.f, 0.f, 0.f, 0.f}; acc[i][1] = acc[i][0]; pin_v(acc[i][0]); pin_v(acc[i][1]); }
  bf16x8 X[2][2][3], W[2][2][2];
  auto ld = [&](auto SET, LAS const char* sb) {
    constexpr int st = decltype(SET)::value;
#pragma unroll
    for (int s = 0; s < 2; ++s) {
      X[st][s][0] = ld_frag(sb + aoff[s]);
      W[st][s][0] = ld_tr(sb + bE + s * 4096, sb + bE + s * 4096 + 512);
      W[st][s][1] = ld_tr(sb + bO + s * 4096, sb + bO + s * 4096 + 512);
      X[st][s][1] = ld_frag(sb + aoff[s] + 2048);
      X[st][s][2] = ld_frag(sb + aoff[s] + 4096);
    }
  };
  auto mm = [&](auto SET) {
    constexpr int st = decltype(SET)::value;
#pragma unroll
    for (int s = 0; s < 2; ++s)
#pragma unroll
      for (int i = 0; i < 3; ++i) { acc[i][0] = mfma(W[st][s][0], X[st][s][i], acc[i][0]); acc[i][1] = mfma(W[st][s][1], X[st][s][i], acc[i][1]); }
  };
  consumer_loop_full<NC, T_NS, T_SLOT, 12, 14>(lds, ld, mm, [&]() { TS(ws, kk, 1); });
  TS(ws, kk, 2);
}

template <int NC, bool SHIFT>
DEVI void t96_loader(LAS char* lds, int w, int lane, const char* A, unsigned lda, int row0, const char* B, unsigned ldb, int rot) {
  unsigned voffA[3], voffB[2];
  const int pslot = lane & 7;
  int pcs[3];
#pragma unroll
  for (int i = 0; i < 3; ++i) {
    int row = (w + 4 * i) * 8 + (lane >> 3), pc = pslot ^ ((row >> 1) & 7);
    int gr = row0 + row; if (gr > S_ - 1) gr = S_ - 1;
    voffA[i] = gr * lda + pc * 16; pcs[i] = pc * 8;
  }
#pragma unroll
  for (int i = 0; i < 2; ++i) {
    int k = (w + 4 * i) * 8 + (lane >> 3);
    int sp = pslot ^ ((((k >> 1) & 1) << 2) | ((k >> 3) & 1));
    voffB[i] = k * ldb + sp * 16;
  }
  auto issue = [&](int k, int slot) {
    int c = k + rot; if (c >= NC) c -= NC;
    const char* pa = pin_s(A + c * 128);
    const char* pb = pin_s(B + (size_t)c * 64 * ldb);
    LAS char* sb = lds + slot * T_SLOT + w * 1024;
#pragma unroll
    for (int i = 0; i < 3; ++i) {
      unsigned vo = voffA[i];
      if constexpr (SHIFT) {
        int kc = c * 64 + pcs[i];
        vo += (kc < 160) ? LD_A : (kc < 320) ? 3 * LD_A : (kc < 480) ? 0 : 4 * LD_A;
      }
      glds16(pa + vo, sb + i * 4096);
    }
#pragma unroll
    for (int i = 0; i < 2; ++i) glds16(pb + voffB[i], sb + T_AB + i * 4096);
  };
  loader_loop<T_NS, NC, 5, T_R, 2, 0>(issue, []() {});
}

DEVI void pair8(const f32x4& e, const f32x4& o, int g, f32x2 (&v)[4]) {
  const bool od = g & 1;
  v[0] = od ? lo2(o) : lo2(e); v[1] = od ? hi2(o) : hi2(e); v[2] = od ? lo2(e) : lo2(o); v[3] = od ? hi2(e) : hi2(o);
}
DEVI void t96_map(int tile, int& panel, int& ct, int& pin) {
  int grp = tile / 40, rem = tile - grp * 40;
  if (grp < 5) { ct = rem >> 2; pin = rem & 3; } else { ct = rem >> 1; pin = rem & 1; }
  panel = grp * 4 + pin;
}

struct K2Args { const char* x; const char* w2; char* ws; };

DEVI void k2_side(const K2Args& a, LAS char* lds, int wid, int lane, int hg, int tq) {
  const int tid = wid * 64 + lane;
  const char* src = a.ws + WS_PQ + ((size_t)hg * 2048 + tq * 512) * 160;
#pragma unroll
  for (int i = 0; i < 10; ++i) glds16(src + (size_t)((wid * 10 + i) * 1024 + lane * 16), lds + (wid * 10 + i) * 1024);
  LAS float* kvs = (LAS float*)(lds + 81920);
  LAS char* frg = lds + 81920 + 6720;
  if (tid < 420) {
    const f32x4* p = (const f32x4*)(a.ws + WS_KVP) + (size_t)hg * 420 + tid;
    f32x4 v[16];
#pragma unroll
    for (int i = 0; i < 16; ++i) v[i] = p[(size_t)i * 32 * 105];
#pragma unroll
    for (int i = 0; i < 8; ++i) v[i] += v[i + 8];
#pragma unroll
    for (int i = 0; i < 4; ++i) v[i] += v[i + 4];
    *(LAS f32x4*)(kvs + tid * 4) = (v[0] + v[1]) + (v[2] + v[3]);
  }
  LGKM0();
  BAR();
  const int g = lane >> 4, t = lane & 15;
  if (wid < 4) {
    LAS const float* kh = kvs + wid * 420;
    u32x4 u0, u1, uz;
#pragma unroll
    for (int e = 0; e < 4; ++e) {
      int d0 = 8 * g + 2 * e, d1 = d0 + 1;
      bool v = d0 < 20;
      int dd0 = v ? d0 : 0, dd1 = v ? d1 : 0;
      float a0 = kh[dd0 * 20 + t], a1 = kh[dd1 * 20 + t];
      float b0 = kh[dd0 * 20 + 16 + (t & 3)], b1 = kh[dd1 * 20 + 16 + (t & 3)];
      float z0 = kh[400 + dd0], z1 = kh[400 + dd1];
      u0[e] = v ? pack2(a0, a1) : 0u;
      u1[e] = (v && t < 4) ? pack2(b0, b1) : 0u;
      uz[e] = v ? pack2(z0, z1) : 0u;
    }
    *(LAS u32x4*)(frg + (wid * 3 + 0) * 1024 + lane * 16) = u0;
    *(LAS u32x4*)(frg + (wid * 3 + 1) * 1024 + lane * 16) = u1;
    *(LAS u32x4*)(frg + (wid * 3 + 2) * 1024 + lane * 16) = uz;
  }
  WAITV(0);
  LGKM0();
  BAR();
  bf16x8 A[4][3];
#pragma unroll
  for (int j = 0; j < 4; ++j)
#pragma unroll
    for (int k = 0; k < 3; ++k) A[j][k] = ld_frag(frg + (j * 3 + k) * 1024 + lane * 16);
#pragma unroll
  for (int i = 0; i < 4; ++i) {
    LAS char* row0 = lds + (wid * 64 + i * 16 + t) * 160;
    bf16x8 Bq[4];
#pragma unroll
    for (int j = 0; j < 4; ++j) {
      LAS char* row = row0 + 40 * j;
      u32x2 lo = *(LAS const u32x2*)(row + 16 * (g < 3 ? g : 0)), hi = *(LAS const u32x2*)(row + 16 * (g < 2 ? g : 0) + 8);
      u32x4 ub = {g < 3 ? lo[0] : 0u, g < 3 ? lo[1] : 0u, g < 2 ? hi[0] : 0u, g < 2 ? hi[1] : 0u};
      Bq[j] = __builtin_bit_cast(bf16x8, ub);
    }
    CBAR();
#pragma unroll
    for (int j = 0; j < 4; ++j) {
      LAS char* row = row0 + 40 * j;
      const f32x4 z4 = {0.f, 0.f, 0.f, 0.f};
      f32x4 d0 = mfma(A[j][0], Bq[j], z4), d1 = mfma(A[j][1], Bq[j], z4), dz = mfma(A[j][2], Bq[j], z4);
      float inv = __builtin_amdgcn_rcpf(dz[0] + 1e-6f);
      *(LAS u32x2*)(row + 8 * g) = u32x2{pack2v(lo2(d0) * inv), pack2v(hi2(d0) * inv)};
      if (g == 0) *(LAS u32x2*)(row + 32) = u32x2{pack2v(lo2(d1) * inv), pack2v(hi2(d1) * inv)};
    }
  }
  const rsrc_t rw = mk_rsrc(a.ws, WS_END);
  const unsigned dst = WS_O + (tq * 512 + wid * 64) * LD_A + hg * 160;
  LGKM0();
#pragma unroll
  for (int i = 0; i < 10; ++i) {
    int P = i * 64 + lane, r = P / 10, qq = P - r * 10;
    u32x4 v = *(LAS const u32x4*)(lds + (wid * 64) * 160 + P * 16);
    st16<ST_AUX>(rw, dst + r * LD_A + qq * 16, v);
  }
}

__global__ __launch_bounds__(512) void k2_kernel(K2Args a) {
  extern __shared__ __attribute__((aligned(16))) char smem[];
  LAS char* lds = (LAS char*)smem;
  const int wid = __builtin_amdgcn_readfirstlane(threadIdx.x >> 6), lane = threadIdx.x & 63;
  const int xcd = blockIdx.x & 7, j = blockIdx.x >> 3;
  TS(a.ws, 1, 0);
  if (j >= 28) { int sidx = (j - 28) * 8 + xcd; k2_side(a, lds, wid, lane, sidx & 7, sidx >> 3); TS(a.ws, 1, 3); return; }
  const int tile = xcd * 28 + j;
  if (tile >= 220) return;
  int panel, ct, pin;
  t96_map(tile, panel, ct, pin);
  if (wid >= 4) {
    t96_loader<10, true>(lds, wid - 4, lane, a.ws + WS_HPAD, LD_A, panel * 96, a.w2 + ct * 128, 1280, USE_ROT ? (ct + (pin * 5) / 2) % 10 : 0);
    return;
  }
  const int wr = wid >> 1, wc = wid & 1, g = lane >> 4, t = lane & 15;
  const rsrc_t rx = mk_rsrc(a.x, S_ * 1280), rw = mk_rsrc(a.ws, WS_END);
  u32x4 xv[3]; f32x2 st[3]; int tok[3];
#pragma unroll
  for (int i = 0; i < 3; ++i) {
    tok[i] = panel * 96 + wr * 48 + i * 16 + t;
    int tk = tok[i] < S_ ? tok[i] : S_ - 1;
    xv[i] = ld16(rx, tk * 1280 + ct * 128 + wc * 64 + g * 16);
    st[i] = ld8f(rw, WS_XST + tk * 8);
  }
  f32x4 acc[3][2];
  t96_consumer<10>(lds, wid, lane, acc, a.ws, 1);
#pragma unroll
  for (int i = 0; i < 3; ++i) {
    f32x2 v[4];
    pair8(acc[i][0], acc[i][1], g, v);
    u32x4 o; float s1 = 0.f, s2 = 0.f;
#pragma unroll
    for (int e = 0; e < 4; ++e) {
      unsigned xn = pack2v((unpack2(xv[i][e]) - st[i][0]) * st[i][1]);
      o[e] = pack2v(unpack2(xn) + v[e]);
      s1 = dot2acc(o[e], 0x3f803f80u, s1); s2 = dot2acc(o[e], o[e], s2);
    }
    s1 = red_g(s1); s2 = red_g(s2);
    if (tok[i] < S_) {
      st16<ST_AUX>(rw, WS_X1 + tok[i] * LD_A + ct * 128 + wc * 64 + g * 16, o);
      if (g == 0) st8f<ST_AUX>(rw, WS_X1P + ((2 * ct + wc) * 2048 + tok[i]) * 8, f32x2{s1, s2});
    }
  }
  TS(a.ws, 1, 3);
}

constexpr int K3_NS = 4, K3_AB = 144 * 128, K3_BB = 64 * 256, K3_SLOT = K3_AB + K3_BB;
constexpr int K3_TAB = K3_NS * K3_SLOT;
constexpr int K3_LDS = K3_TAB + 144 * 8;

struct K3Args { const char* w3; const char* wo; char* ws; };

template <bool MLP>
DEVI void k3_consumer(const K3Args& a, LAS char* lds, int wid, int lane, int panel, int ct) {
  const int g = lane >> 4, t = lane & 15, sw = t >> 1;
  LAS f32x2* stat = (LAS f32x2*)(lds + K3_TAB);
  const rsrc_t rw = mk_rsrc(a.ws, WS_END);
  f32x2 part[20];
  const int tid = wid * 64 + lane;
  if constexpr (MLP) {
    int tk = panel * 144 + (tid < 144 ? tid : 0); if (tk > S_ - 1) tk = S_ - 1;
    if (wid < 3) {
#pragma unroll
      for (int i = 0; i < 20; ++i) part[i] = ld8f(rw, WS_X1P + (i * 2048 + tk) * 8);
    }
  }
  unsigned aoff[2];
#pragma unroll
  for (int s = 0; s < 2; ++s) aoff[s] = t * 128 + (((4 * s + g) ^ sw) * 16);
  const int q = t >> 2, p = lane & 3;
  const int swz = (q << 2) | (g & 1);
  const unsigned rowb = K3_AB + (8 * g + q) * 256 + (((4 * wid + p) ^ swz) * 16);
  const unsigned bE = rowb + ((p & 1) ? 8 : 0), bO = rowb + ((p & 1) ? 0 : 8);
  f32x4 acc[9][2];
#pragma unroll
  for (int i = 0; i < 9; ++i) { acc[i][0] = f32x4{0.f, 0.f, 0.f, 0.f}; acc[i][1] = acc[i][0]; pin_v(acc[i][0]); pin_v(acc[i][1]); }
  f32x4 cE = {0.f, 0.f, 0.f, 0.f}, cO = cE;
  bf16x8 ones8 = __builtin_bit_cast(bf16x8, u32x4{0x3f803f80u, 0x3f803f80u, 0x3f803f80u, 0x3f803f80u});
  if constexpr (MLP) { pin_v(cE); pin_v(cO); pin_v(ones8); }
  bf16x8 X[2][9], W[2][2];
  auto ld = [&](auto SET, LAS const char* sb, auto SS) {
    constexpr int st = decltype(SET)::value, s = decltype(SS)::value;
    X[st][0] = ld_frag(sb + aoff[s]);
    W[st][0] = ld_tr(sb + bE + s * 8192, sb + bE + s * 8192 + 1024);
    W[st][1] = ld_tr(sb + bO + s * 8192, sb + bO + s * 8192 + 1024);
#pragma unroll
    for (int j = 1; j < 9; ++j) X[st][j] = ld_frag(sb + aoff[s] + j * 2048);
  };
  auto mm = [&](auto SET) {
    constexpr int st = decltype(SET)::value;
#pragma unroll
    for (int i = 0; i < 9; ++i) { acc[i][0] = mfma(W[st][0], X[st][i], acc[i][0]); acc[i][1] = mfma(W[st][1], X[st][i], acc[i][1]); }
    if constexpr (MLP) { cE = mfma(W[st][0], ones8, cE); cO = mfma(W[st][1], ones8, cO); }
  };
  auto pre = [&]() {
    TS(a.ws, 2, 1);
    if constexpr (MLP) {
      if (wid < 3) {
#pragma unroll
        for (int i = 0; i < 10; ++i) part[i] += part[i + 10];
#pragma unroll
        for (int i = 0; i < 5; ++i) part[i] += part[i + 5];
        f32x2 s = (part[0] + part[1]) + (part[2] + part[3]) + part[4];
        float m = s[0] * (1.0f / 640.0f);
        float var = fmaxf(s[1] * (1.0f / 640.0f) - m * m, 0.f);
        if (tid < 144) stat[tid] = f32x2{m, __builtin_amdgcn_rsqf(var + 1e-5f)};
      }
    }
  };
  consumer_loop<10, K3_NS, K3_SLOT, MLP ? 20 : 18, 13, 0>(lds, ld, mm, pre);
  TS(a.ws, 2, 2);
  const unsigned obase = MLP ? WS_HID + ct * 256 + wid * 64 + g * 16 : WS_S + (ct - 12) * 256 + wid * 64 + g * 16;
  const unsigned rs = MLP ? LD_HID : 1280;
  const bool od = g & 1;
#pragma unroll
  for (int i = 0; i < 9; ++i) {
    const int tok = panel * 144 + i * 16 + t;
    f32x2 e0 = lo2(acc[i][0]), e1 = hi2(acc[i][0]), o0 = lo2(acc[i][1]), o1 = hi2(acc[i][1]);
    if constexpr (MLP) {
      const f32x2 st = stat[i * 16 + t];
      e0 = gelu2((e0 - lo2(cE) * st[0]) * st[1]); e1 = gelu2((e1 - hi2(cE) * st[0]) * st[1]);
      o0 = gelu2((o0 - lo2(cO) * st[0]) * st[1]); o1 = gelu2((o1 - hi2(cO) * st[0]) * st[1]);
    }
    unsigned pe0 = pack2v(e0), pe1 = pack2v(e1), po0 = pack2v(o0), po1 = pack2v(o1);
    if (tok < S_) st16<ST_AUX>(rw, obase + tok * rs, od ? u32x4{po0, po1, pe0, pe1} : u32x4{pe0, pe1, po0, po1});
  }
}

__global__ __launch_bounds__(512) void k3_kernel(K3Args a) {
  extern __shared__ __attribute__((aligned(16))) char smem[];
  LAS char* lds = (LAS char*)smem;
  const int wid = __builtin_amdgcn_readfirstlane(threadIdx.x >> 6), lane = threadIdx.x & 63;
  const int xcd = blockIdx.x & 7, j = blockIdx.x >> 3;
  const int tile = xcd * 32 + j;
  if (tile >= 255) return;
  const int grp = tile / 68, rem = tile - grp * 68;
  int ct, pin;
  if (grp < 3) { ct = rem >> 2; pin = rem & 3; } else { ct = rem / 3; pin = rem - ct * 3; }
  const int panel = grp * 4 + pin;
  const bool mlp = ct < 12;
  TS(a.ws, 2, 0);
  if (wid >= 4) {
    const int w = wid - 4;
    const char* A = mlp ? a.ws + WS_X1 : a.ws + WS_O;
    const char* B = mlp ? a.w3 + ct * 256 : a.wo + (ct - 12) * 256;
    const unsigned ldb = mlp ? 3072 : 1280;
    unsigned voffA[5], voffB[4];
#pragma unroll
    for (int i = 0; i < 5; ++i) {
      int ii = w + 4 * i; if (ii > 17) ii = 17;
      int row = ii * 8 + (lane >> 3), pc = (lane & 7) ^ ((row >> 1) & 7);
      int gr = panel * 144 + row; if (gr > S_ - 1) gr = S_ - 1;
      voffA[i] = gr * LD_A + pc * 16;
    }
#pragma unroll
    for (int i = 0; i < 4; ++i) {
      int P = (w + 4 * i) * 64 + lane, k = P >> 4, qq = P & 15;
      int sp = qq ^ (((k & 3) << 2) | ((k >> 3) & 1));
      voffB[i] = k * ldb + sp * 16;
    }
    const int last = (w + 16 > 17) ? 17 : w + 16;
    const int rot = USE_ROT ? ((pin * 5) / 2 + ((ct & 7) * 5) / 4) % 10 : 0;
    auto issue = [&](int k, int slot) {
      int c = k + rot; if (c >= 10) c -= 10;
      const char* pa = pin_s(A + c * 128);
      const char* pb = pin_s(B + (size_t)c * 64 * ldb);
      LAS char* sb = lds + slot * K3_SLOT;
#pragma unroll
      for (int i = 0; i < 4; ++i) glds16(pa + voffA[i], sb + (w + 4 * i) * 1024);
      glds16(pa + voffA[4], sb + last * 1024);
#pragma unroll
      for (int i = 0; i < 4; ++i) glds16(pb + voffB[i], sb + K3_AB + (w + 4 * i) * 1024);
    };
    loader_loop<K3_NS, 10, 9, 2, 1, 0>(issue, []() {});
    return;
  }
  if (mlp) k3_consumer<true>(a, lds, wid, lane, panel, ct);
  else k3_consumer<false>(a, lds, wid, lane, panel, ct);
  TS(a.ws, 2, 3);
}

struct K4Args { const char* x; const char* w4; char* ws; char* out; };

__global__ __launch_bounds__(512) void k4_kernel(K4Args a) {
  extern __shared__ __attribute__((aligned(16))) char smem[];
  LAS char* lds = (LAS char*)smem;
  const int wid = __builtin_amdgcn_readfirstlane(threadIdx.x >> 6), lane = threadIdx.x & 63;
  const int xcd = blockIdx.x & 7, j = blockIdx.x >> 3;
  const int tile = xcd * 28 + j;
  if (j >= 28 || tile >= 220) return;
  int panel, ct, pin;
  t96_map(tile, panel, ct, pin);
  TS(a.ws, 3, 0);
  if (wid >= 4) {
    t96_loader<24, false>(lds, wid - 4, lane, a.ws + WS_HID, LD_HID, panel * 96, a.w4 + ct * 128, 1280, USE_ROT ? (pin * 6 + (ct * 24) / 7) % 24 : 0);
    return;
  }
  const int wr = wid >> 1, wc = wid & 1, g = lane >> 4, t = lane & 15;
  const rsrc_t rx = mk_rsrc(a.x, S_ * 1280), rw = mk_rsrc(a.ws, WS_END), ro = mk_rsrc(a.out, S_ * 1280);
  u32x4 xv[3], x1v[3], sv[3]; int tok[3];
#pragma unroll
  for (int i = 0; i < 3; ++i) {
    tok[i] = panel * 96 + wr * 48 + i * 16 + t;
    int tk = tok[i] < S_ ? tok[i] : S_ - 1;
    unsigned off = tk * 1280 + ct * 128 + wc * 64 + g * 16;
    xv[i] = ld16(rx, off);
    x1v[i] = ld16(rw, WS_X1 + tk * LD_A + ct * 128 + wc * 64 + g * 16);
    sv[i] = ld16(rw, WS_S + off);
  }
  f32x4 acc[3][2];
  t96_consumer<24>(lds, wid, lane, acc, a.ws, 3);
#pragma unroll
  for (int i = 0; i < 3; ++i) {
    f32x2 v[4];
    pair8(acc[i][0], acc[i][1], g, v);
    u32x4 o;
#pragma unroll
    for (int e = 0; e < 4; ++e) o[e] = pack2v(unpack2(xv[i][e]) + unpack2(sv[i][e]) * silu2(unpack2(x1v[i][e]) + v[e]));
    st16<OUT_AUX>(ro, tok[i] * 1280 + ct * 128 + wc * 64 + g * 16, o);
  }
  TS(a.ws, 3, 3);
}

extern "C" void kernel_launch(void* const* d_in, const int* in_sizes, int n_in, void* d_out, int out_size, void* d_ws, size_t ws_size,
                              hipStream_t stream) {
  const char* x = (const char*)d_in[0];
  const char* wqkv = (const char*)d_in[1];
  const char* wo = (const char*)d_in[2];
  const char* w1 = (const char*)d_in[4];
  const char* w2 = (const char*)d_in[5];
  const char* w3 = (const char*)d_in[6];
  const char* w4 = (const char*)d_in[7];
  char* ws = (char*)d_ws;
  (void)hipFuncSetAttribute((const void*)k1_kernel, hipFuncAttributeMaxDynamicSharedMemorySize, K1_LDS);
  (void)hipFuncSetAttribute((const void*)k2_kernel, hipFuncAttributeMaxDynamicSharedMemorySize, T_LDS);
  (void)hipFuncSetAttribute((const void*)k3_kernel, hipFuncAttributeMaxDynamicSharedMemorySize, K3_LDS);
  (void)hipFuncSetAttribute((const void*)k4_kernel, hipFuncAttributeMaxDynamicSharedMemorySize, T_LDS);
  k1_kernel<<<256, 512, K1_LDS, stream>>>(K1Args{x, wqkv, w1, w2, w3, w4, wo, ws});
  k2_kernel<<<256, 512, T_LDS, stream>>>(K2Args{x, w2, ws});
  k3_kernel<<<256, 512, K3_LDS, stream>>>(K3Args{w3, wo, ws});
  k4_kernel<<<256, 512, T_LDS, stream>>>(K4Args{x, w4, ws, (char*)d_out});
}
```

```cpp
#include <hip/hip_runtime.h>
#include <stdint.h>

#define LAS __attribute__((address_space(3)))
#define GAS __attribute__((address_space(1)))
#define DEVI __device__ __forceinline__

typedef __bf16 bf16x2 __attribute__((ext_vector_type(2)));
typedef __bf16 bf16x8 __attribute__((ext_vector_type(8)));
typedef float f32x2 __attribute__((ext_vector_type(2)));
typedef float f32x4 __attribute__((ext_vector_type(4)));
typedef short s16x4 __attribute__((ext_vector_type(4)));
typedef unsigned u32x2 __attribute__((ext_vector_type(2)));
typedef unsigned u32x4 __attribute__((ext_vector_type(4)));
typedef __amdgpu_buffer_rsrc_t rsrc_t;

#define ST_AUX 16
#define OUT_AUX 0
#define USE_ROT 0
#define USE_PF 1

#define T_R 4
#define ROT1 0
#define PF1 1
constexpr int S_ = 2048;

#define LD_A 1408
#define LD_HID 3200
constexpr unsigned WS_HPAD = 0;
constexpr unsigned WS_PQ   = WS_HPAD + 2052 * LD_A;
constexpr unsigned WS_KVP  = WS_PQ + 2048 * 640 * 2;
constexpr unsigned WS_XST  = WS_KVP + 16 * 32 * 420 * 4;
constexpr unsigned WS_X1   = WS_XST + 2048 * 8;
constexpr unsigned WS_X1P  = WS_X1 + 2048 * LD_A;
constexpr unsigned WS_O    = WS_X1P + 20 * 2048 * 8;
constexpr unsigned WS_HID  = WS_O + 2048 * LD_A;
constexpr unsigned WS_S    = WS_HID + 2048 * LD_HID;
constexpr unsigned WS_END  = WS_S + 2048 * 640 * 2;

template <int V> struct IC { static constexpr int value = V; };
DEVI unsigned pack2(float a, float b) { f32x2 v = {a, b}; return __builtin_bit_cast(unsigned, __builtin_convertvector(v, bf16x2)); }
DEVI unsigned pack2v(f32x2 v) { return __builtin_bit_cast(unsigned, __builtin_convertvector(v, bf16x2)); }
DEVI float bflo(unsigned u) { return __builtin_bit_cast(float, u << 16); }
DEVI float bfhi(unsigned u) { return __builtin_bit_cast(float, u & 0xffff0000u); }
DEVI f32x2 unpack2(unsigned u) { return f32x2{bflo(u), bfhi(u)}; }
DEVI float dot2acc(unsigned a, unsigned b, float c) { return __builtin_amdgcn_fdot2_f32_bf16(__builtin_bit_cast(bf16x2, a), __builtin_bit_cast(bf16x2, b), c, false); }
DEVI f32x2 lo2(f32x4 v) { return f32x2{v[0], v[1]}; }
DEVI f32x2 hi2(f32x4 v) { return f32x2{v[2], v[3]}; }
DEVI f32x2 gelu2(f32x2 u) {
  const float c1 = -2.3022081986f, c2 = -0.10294324f;
  f32x2 w = u * (u * u * c2 + c1);
  f32x2 d = f32x2{__builtin_amdgcn_exp2f(w[0]), __builtin_amdgcn_exp2f(w[1])} + 1.0f;
  return u * f32x2{__builtin_amdgcn_rcpf(d[0]), __builtin_amdgcn_rcpf(d[1])};
}
DEVI f32x2 silu2(f32x2 b) {
  f32x2 w = b * -1.4426950409f;
  f32x2 d = f32x2{__builtin_amdgcn_exp2f(w[0]), __builtin_amdgcn_exp2f(w[1])} + 1.0f;
  return b * f32x2{__builtin_amdgcn_rcpf(d[0]), __builtin_amdgcn_rcpf(d[1])};
}
DEVI f32x2 phi2(f32x2 t) {
  f32x2 w = t * 1.4426950409f;
  f32x2 e = {__builtin_amdgcn_exp2f(fminf(w[0], 0.f)), __builtin_amdgcn_exp2f(fminf(w[1], 0.f))};
  return (f32x2{fmaxf(t[0], 0.f), fmaxf(t[1], 0.f)} + e) + 1e-6f;
}
DEVI float red_g(float v) { v += __shfl_xor(v, 16); v += __shfl_xor(v, 32); return v; }
DEVI f32x4 mfma(bf16x8 a, bf16x8 b, f32x4 c) { return __builtin_amdgcn_mfma_f32_16x16x32_bf16(a, b, c, 0, 0, 0); }
DEVI bf16x8 ld_frag(LAS const char* p) { return *(LAS const bf16x8*)p; }
DEVI bf16x8 ld_tr(LAS const char* p0, LAS const char* p1) {
  s16x4 a = __builtin_amdgcn_ds_read_tr16_b64_v4i16((LAS s16x4*)p0);
  s16x4 b = __builtin_amdgcn_ds_read_tr16_b64_v4i16((LAS s16x4*)p1);
  return __builtin_bit_cast(bf16x8, __builtin_shufflevector(a, b, 0, 1, 2, 3, 4, 5, 6, 7));
}
DEVI void glds16(const char* g, LAS char* l) { __builtin_amdgcn_global_load_lds((GAS const void*)g, (LAS void*)l, 16, 0, 0); }
DEVI rsrc_t mk_rsrc(const void* p, unsigned bytes) { return __builtin_amdgcn_make_buffer_rsrc((void*)p, 0, (int)bytes, 0x00020000); }
template <int AUX> DEVI void st16(rsrc_t r, unsigned off, u32x4 v) { __builtin_amdgcn_raw_buffer_store_b128(v, r, off, 0, AUX); }
template <int AUX> DEVI void st8(rsrc_t r, unsigned off, u32x2 v) { __builtin_amdgcn_raw_buffer_store_b64(v, r, off, 0, AUX); }
template <int AUX> DEVI void st8f(rsrc_t r, unsigned off, f32x2 v) { __builtin_amdgcn_raw_buffer_store_b64(__builtin_bit_cast(u32x2, v), r, off, 0, AUX); }
template <int AUX> DEVI void st4f(rsrc_t r, unsigned off, float v) { __builtin_amdgcn_raw_buffer_store_b32(__builtin_bit_cast(unsigned, v), r, off, 0, AUX); }
DEVI u32x4 ld16(rsrc_t r, unsigned off) { return __builtin_amdgcn_raw_buffer_load_b128(r, off, 0, 0); }
DEVI f32x2 ld8f(rsrc_t r, unsigned off) { return __builtin_bit_cast(f32x2, __builtin_amdgcn_raw_buffer_load_b64(r, off, 0, 0)); }
#define CBAR() asm volatile("" ::: "memory")
#define BAR() do { CBAR(); __builtin_amdgcn_s_barrier(); CBAR(); } while (0)
#define SB0() __builtin_amdgcn_sched_barrier(0)
#define LGKM0() __builtin_amdgcn_s_waitcnt(0xC07F)
#define WAITV(n) asm volatile("s_waitcnt vmcnt(%0)" ::"i"(n) : "memory")
DEVI const char* pin_s(const char* p) { asm volatile("" : "+s"(p)); return p; }
template <class T> DEVI void pin_v(T& v) { asm volatile("" : "+v"(v)); }

#define PFC 1
#define CODE_ADDR(sym) ({ unsigned lo_, hi_; asm volatile("s_getpc_b64 s[40:41]\n\ts_add_u32 s40, s40, " #sym "@rel32@lo+4\n\ts_addc_u32 s41, s41, " #sym "@rel32@hi+12\n\ts_mov_b32 %0, s40\n\ts_mov_b32 %1, s41" : "=s"(lo_), "=s"(hi_) : : "s40", "s41", "scc"); (const char*)(((unsigned long long)hi_ << 32) | lo_); })
DEVI void prefetch_code(const char* beg, const char* end, int idx, LAS char* sink) {
  long len = end - beg;
  if ((long)idx * 128 < len) __builtin_amdgcn_global_load_lds((GAS const void*)(beg + idx * 128), (LAS void*)sink, 4, 0, 0);
}

#define TS(ws, k, j) do {} while (0)

template <int NS, int NC, int NI, int R, int LAG, int XI, class Issue, class Hook>
DEVI void loader_loop(Issue&& issue_, Hook&& hook) {
  auto& issue = issue_;
#pragma unroll
  for (int k = 0; k < R; ++k) issue(k, k);
  WAITV((R - 1) * NI);
  BAR();
#pragma unroll
  for (int k = R; k < NS; ++k) issue(k, k);
#pragma unroll
  for (int k = 1; k < LAG; ++k) { WAITV((NS - 1 - k) * NI); BAR(); }
  int slot = 0;
#pragma unroll 1
  for (int k = LAG; k <= NC - NS + LAG - 1; ++k) {
    WAITV((NS - LAG - 1) * NI);
    BAR();
    issue(k - LAG + NS, slot);
    slot = (slot + 1 == NS) ? 0 : slot + 1;
  }
  hook();
#pragma unroll
  for (int k = NC - NS + LAG; k < NC; ++k) {
    WAITV((NC - k - 1) * NI + XI);
    BAR();
  }
}

template <int NM, int ND, int NV>
DEVI void sched_region() {
  constexpr int DPER = (ND + NM - 3) / (NM - 2);
  constexpr int VPER = (NV + NM - 1) / NM;
#pragma unroll
  for (int i = 0; i < NM; ++i) {
    __builtin_amdgcn_sched_group_barrier(0x008, 1, 0);
    if (i * DPER < ND) {
      __builtin_amdgcn_sched_group_barrier(0x002, 1, 0);
      __builtin_amdgcn_sched_group_barrier(0x100, DPER, 0);
    }
    if (VPER > 0) __builtin_amdgcn_sched_group_barrier(0x002, VPER, 0);
    __builtin_amdgcn_sched_group_barrier(0x004, 1, 0);
  }
}

template <int NC, int NS, int SLOT, int NM, int ND, int NV, class LD, class MM, class PRE>
DEVI void consumer_loop(LAS const char* lds, LD&& ld_, MM&& mm_, PRE&& pre) {
  auto& ld = ld_; auto& mm = mm_;
  BAR();
  ld(IC<0>{}, lds, IC<0>{});
  SB0();
  pre();
  SB0();
  LAS const char* sb = lds;
#pragma unroll 1
  for (int c = 0; c < NC - 1; ++c) {
    ld(IC<1>{}, sb, IC<1>{});
    mm(IC<0>{});
    sb = (sb == lds + (NS - 1) * SLOT) ? lds : sb + SLOT;
    sched_region<NM, ND, NV>();
    SB0();
    LGKM0();
    BAR();
    SB0();
    ld(IC<0>{}, sb, IC<0>{});
    mm(IC<1>{});
    sched_region<NM, ND, NV>();
    SB0();
  }
  ld(IC<1>{}, sb, IC<1>{});
  mm(IC<0>{});
  sched_region<NM, ND, NV>();
  SB0();
  mm(IC<1>{});
}

template <int NC, int NS, int SLOT, int NM, int ND, class LD, class MM, class PRE>
DEVI void consumer_loop_full(LAS const char* lds, LD&& ld, MM&& mm, PRE&& pre) {
  BAR();
  ld(IC<0>{}, lds);
  SB0();
  pre();
  SB0();
  LAS const char* s1 = lds + SLOT;
#pragma unroll 1
  for (int c = 0; c + 2 < NC; c += 2) {
    LAS const char* s2 = (s1 == lds + (NS - 1) * SLOT) ? lds : s1 + SLOT;
    BAR();
    SB0();
    ld(IC<1>{}, s1);
    mm(IC<0>{});
    s1 = (s2 == lds + (NS - 1) * SLOT) ? lds : s2 + SLOT;
    sched_region<NM, ND, 0>();
    SB0();
    BAR();
    SB0();
    ld(IC<0>{}, s2);
    mm(IC<1>{});
    sched_region<NM, ND, 0>();
    SB0();
  }
  BAR();
  SB0();
  ld(IC<1>{}, s1);
  mm(IC<0>{});
  sched_region<NM, ND, 0>();
  SB0();
  mm(IC<1>{});
}

constexpr int K1_BM = 128, K1_BN = 160, K1_NS = 4;
constexpr int K1_AB = K1_BM * 128, K1_BB = 64 * K1_BN * 2, K1_SLOT = K1_AB + K1_BB;
constexpr int K1_TAB = K1_NS * K1_SLOT;
constexpr int K1_DUMMY = K1_TAB + 128 * 8 + 160 * 4;
constexpr int K1_LDS = K1_DUMMY + 256;

struct K1Args { const char* x; const char* wqkv; const char* w1; const char* w2; const char* w3; const char* w4; const char* wo; char* ws; };

template <int MODE>
DEVI void k1_consumer(const K1Args& a, LAS char* lds, int wid, int lane, int panel, int ct) {
  constexpr bool KV = (MODE == 1);
  const int wr = wid >> 1, wc = wid & 1, g = lane >> 4, t = lane & 15;
  const int sw = t >> 1;
  unsigned aoff[2][2];
#pragma unroll
  for (int s = 0; s < 2; ++s)
#pragma unroll
    for (int hf = 0; hf < 2; ++hf) aoff[s][hf] = (wr * 64 + ((2 * hf + 2 * wc) & 3) * 16 + t) * 128 + (((4 * s + g) ^ sw) * 16);
  const int q = t >> 2, p = lane & 3;
  unsigned boff[5];
  if constexpr (KV) {
    const unsigned rowb = (8 * g + q) * 320 + 8 * p;
    const int odd = g & 1;
    boff[0] = K1_AB + rowb + 160 * wc + (odd ? ((wc & 1) ? -32 : 32) : 0);
    boff[1] = K1_AB + rowb + 160 * wc + (odd ? ((wc & 1) ? 32 : -32) : 0);
    boff[2] = boff[3] = boff[4] = 0;
  } else {
    const unsigned rowb = K1_AB + (8 * g + q) * 320 + 160 * wc;
    const int pp = p ^ (g & 1);
    const int pr0 = wr ? 64 : 0, pr1 = wr ? 0 : 64;
    const unsigned bE = rowb + pp * 16 + ((p & 1) ? 8 : 0), bO = rowb + pp * 16 + ((p & 1) ? 0 : 8);
    boff[0] = bE + pr0; boff[1] = bO + pr0; boff[2] = bE + pr1; boff[3] = bO + pr1;
    boff[4] = rowb + 128 + (((p >> 1) ^ (g & 1)) * 16) + (p & 1) * 8;
  }
  unsigned el[7];
  if constexpr (KV) {
#pragma unroll
    for (int i = 0; i < 7; ++i) {
      int idx = wid * 64 + lane + 256 * i;
      int hl = idx / 420, rem = idx - hl * 420, w2 = hl >> 1, hb = hl & 1;
      int dd, k, col;
      if (rem < 400) {
        dd = rem / 20; int e = rem - dd * 20;
        int kc = 20 * hb + dd, vc = 40 + 20 * hb + e;
        int fa = kc >> 4, fb = vc >> 4;
        k = (fa == 0) ? fb - 2 : (fa == 1) ? fb : fb + 2;
        col = vc & 15; dd = kc & 15;
      } else {
        int kc = 20 * hb + (rem - 400);
        k = 7 + (kc >> 4); col = 0; dd = kc & 15;
      }
      el[i] = (((k * 64 + (dd >> 2) * 16 + col) * 4 + (dd & 3)) + (w2 & 1) * 2560) * 4;
      pin_v(el[i]);
    }
  }

  f32x4 acc[4][5];
#pragma unroll
  for (int i = 0; i < 4; ++i)
#pragma unroll
    for (int f = 0; f < 5; ++f) { acc[i][f] = f32x4{0.f, 0.f, 0.f, 0.f}; pin_v(acc[i][f]); }
  f32x4 s1[2], s2[2], cs[3];
#pragma unroll
  for (int j = 0; j < 2; ++j) { s1[j] = f32x4{0.f, 0.f, 0.f, 0.f}; s2[j] = s1[j]; pin_v(s1[j]); pin_v(s2[j]); }
#pragma unroll
  for (int j = 0; j < 3; ++j) { cs[j] = f32x4{0.f, 0.f, 0.f, 0.f}; pin_v(cs[j]); }
  const unsigned ONES = 0x3f803f80u;
  bf16x8 ones8 = __builtin_bit_cast(bf16x8, u32x4{ONES, ONES, ONES, ONES});
  pin_v(ones8);

  bf16x8 X[2][4], W[2][5];
  auto ld = [&](auto SET, LAS const char* sb, auto SS) {
    constexpr int st = decltype(SET)::value, s = decltype(SS)::value;
    if constexpr (KV) {
      X[st][0] = ld_frag(sb + aoff[s][0]);
      { LAS const char* pb = sb + boff[0] + s * (32 * 320); W[st][0] = ld_tr(pb, pb + 4 * 320); }
      { LAS const char* pb = sb + boff[1] + 32 + s * (32 * 320); W[st][1] = ld_tr(pb, pb + 4 * 320); }
      { LAS const char* pb = sb + boff[0] + 64 + s * (32 * 320); W[st][2] = ld_tr(pb, pb + 4 * 320); }
      { LAS const char* pb = sb + boff[1] + 96 + s * (32 * 320); W[st][3] = ld_tr(pb, pb + 4 * 320); }
      { LAS const char* pb = sb + boff[0] + 128 + s * (32 * 320); W[st][4] = ld_tr(pb, pb + 4 * 320); }
      X[st][1] = ld_frag(sb + aoff[s][0] + 2048);
      X[st][2] = ld_frag(sb + aoff[s][1]);
      X[st][3] = ld_frag(sb + aoff[s][1] + 2048);
    } else {
      X[st][0] = ld_frag(sb + aoff[s][0]);
#pragma unroll
      for (int f = 0; f < 5; ++f) { LAS const char* pb = sb + boff[f] + s * (32 * 320); W[st][f] = ld_tr(pb, pb + 4 * 320); }
      X[st][1] = ld_frag(sb + aoff[s][0] + 2048);
      X[st][2] = ld_frag(sb + aoff[s][1]);
      X[st][3] = ld_frag(sb + aoff[s][1] + 2048);
    }
  };
  auto mm = [&](auto SET) {
    constexpr int st = decltype(SET)::value;
#pragma unroll
    for (int i = 0; i < 4; ++i)
#pragma unroll
      for (int f = 0; f < 5; ++f) acc[i][f] = KV ? mfma(X[st][i], W[st][f], acc[i][f]) : mfma(W[st][f], X[st][i], acc[i][f]);
#pragma unroll
    for (int j = 0; j < 2; ++j) { s1[j] = mfma(ones8, X[st][j], s1[j]); s2[j] = mfma(X[st][j], X[st][j], s2[j]); }
#pragma unroll
    for (int j = 0; j < 3; ++j) {
      if constexpr (KV) cs[j] = mfma(ones8, wr ? W[st][j + 2] : W[st][j], cs[j]);
      else cs[j] = mfma(W[st][j == 2 ? 4 : j], ones8, cs[j]);
    }
  };
  consumer_loop<10, K1_NS, K1_SLOT, 27, 14, KV ? 12 : 0>(lds, ld, mm, [&]() { TS(a.ws, 0, 1); });
  TS(a.ws, 0, 2);

  LAS float* mean = (LAS float*)(lds + K1_TAB);
  LAS float* rstd = mean + 128;
  LAS float* cst = mean + 256;
#pragma unroll
  for (int j = 0; j < 2; ++j) {
    const int r3 = t & 3;
    float a1 = s1[j][0], a2 = r3 == 0 ? s2[j][0] : r3 == 1 ? s2[j][1] : r3 == 2 ? s2[j][2] : s2[j][3];
    float m = a1 * (1.0f / 640.0f);
    float var = fmaxf(a2 * (1.0f / 640.0f) - m * m, 0.f);
    float r = __builtin_amdgcn_rsqf(var + 1e-5f);
    int row = wr * 64 + ((j + 2 * wc) & 3) * 16 + t;
    if (g == (t >> 2)) { mean[row] = m; rstd[row] = r; }
  }
#pragma unroll
  for (int j = 0; j < 3; ++j) {
    if constexpr (KV) {
      if (g == 0) cst[80 * wc + 16 * (j + 2 * wr) + t] = cs[j][0];
    } else {
      int col = (j == 2) ? 80 * wc + 64 + 4 * g : 80 * wc + (wr ? 32 : 0) + 8 * g + 4 * ((j ^ g) & 1);
      if (t == 0) *(LAS f32x4*)(cst + col) = cs[j];
    }
  }
  LGKM0();
  BAR();

  const rsrc_t rw = mk_rsrc(a.ws, WS_END);
  if constexpr (KV) {
    f32x4 M4[4], R4[4];
#pragma unroll
    for (int j = 0; j < 4; ++j) {
      int row = wr * 64 + ((j + 2 * wc) & 3) * 16 + 4 * g;
      M4[j] = *(LAS const f32x4*)(mean + row); R4[j] = *(LAS const f32x4*)(rstd + row);
    }
    bf16x8 op[2][5];
#pragma unroll
    for (int f = 0; f < 5; ++f) {
      const float c = cst[80 * wc + 16 * f + t];
      const bool isk = (f < 2) || (f == 2 && t < 8);
#pragma unroll
      for (int h = 0; h < 2; ++h) {
        u32x4 u;
#pragma unroll
        for (int jj = 0; jj < 2; ++jj) {
          const int j = 2 * h + jj;
          f32x2 y0 = lo2(R4[j]) * (lo2(acc[j][f]) - lo2(M4[j]) * c);
          f32x2 y1 = hi2(R4[j]) * (hi2(acc[j][f]) - hi2(M4[j]) * c);
          if (f <= 2) {
            f32x2 p0 = phi2(y0), p1 = phi2(y1);
            if (f < 2) { y0 = p0; y1 = p1; } else { y0 = isk ? p0 : y0; y1 = isk ? p1 : y1; }
          }
          u[2 * jj] = pack2v(y0); u[2 * jj + 1] = pack2v(y1);
        }
        op[h][f] = __builtin_bit_cast(bf16x8, u);
      }
    }
    const bf16x8 ones = __builtin_bit_cast(bf16x8, u32x4{ONES, ONES, ONES, ONES});
    f32x4 d[10];
    constexpr int pa[7] = {0, 0, 1, 1, 1, 2, 2}, pb[7] = {2, 3, 2, 3, 4, 3, 4};
#pragma unroll
    for (int k = 0; k < 7; ++k) { d[k] = mfma(op[0][pa[k]], op[0][pb[k]], f32x4{0.f, 0.f, 0.f, 0.f}); d[k] = mfma(op[1][pa[k]], op[1][pb[k]], d[k]); }
#pragma unroll
    for (int k = 0; k < 3; ++k) { d[7 + k] = mfma(op[0][k], ones, f32x4{0.f, 0.f, 0.f, 0.f}); d[7 + k] = mfma(op[1][k], ones, d[7 + k]); }
    LAS f32x4* red = (LAS f32x4*)lds;
#pragma unroll
    for (int k = 0; k < 10; ++k) red[(wid * 10 + k) * 64 + lane] = d[k];
    LGKM0();
    BAR();
    const unsigned kvp = WS_KVP + (panel * 32 + (ct - 4) * 4) * 420 * 4 + (wid * 64 + lane) * 4;
#pragma unroll
    for (int i = 0; i < 7; ++i) {
      float v = *(LAS const float*)(lds + el[i]) + *(LAS const float*)(lds + el[i] + 2 * 2560 * 4);
      if (i < 6 || wid * 64 + lane < 1680 - 6 * 256) st4f<ST_AUX>(rw, kvp + i * 1024, v);
    }
  } else {
    constexpr bool isq = (MODE == 0);
    const int pr0 = wr ? 64 : 0, pr1 = wr ? 0 : 64;
    float m4[4], r4[4];
#pragma unroll
    for (int j = 0; j < 4; ++j) { int row = wr * 64 + ((j + 2 * wc) & 3) * 16 + t; m4[j] = mean[row]; r4[j] = rstd[row]; }
    if (ct == 12 && wc == 0 && g == 0) {
#pragma unroll
      for (int j = 0; j < 4; ++j) st8f<ST_AUX>(rw, WS_XST + (panel * 128 + wr * 64 + j * 16 + t) * 8, f32x2{m4[j], r4[j]});
    }
    f32x4 c4[5];
    {
      const int hE = 4 * (g & 1), hO = 4 - hE;
      c4[0] = *(LAS const f32x4*)(cst + 80 * wc + pr0 / 2 + 8 * g + hE);
      c4[1] = *(LAS const f32x4*)(cst + 80 * wc + pr0 / 2 + 8 * g + hO);
      c4[2] = *(LAS const f32x4*)(cst + 80 * wc + pr1 / 2 + 8 * g + hE);
      c4[3] = *(LAS const f32x4*)(cst + 80 * wc + pr1 / 2 + 8 * g + hO);
      c4[4] = *(LAS const f32x4*)(cst + 80 * wc + 64 + 4 * g);
    }
    unsigned obase, rstride;
    if (isq) { obase = WS_PQ + (2 * ct + wc) * (2048 * 160); rstride = 160; }
    else { obase = WS_HPAD + 2 * LD_A + (ct - 12) * 320 + wc * 160; rstride = LD_A; }
    const bool od = g & 1;
#pragma unroll
    for (int j = 0; j < 4; ++j) {
      const int tok = panel * 128 + wr * 64 + ((j + 2 * wc) & 3) * 16 + t;
      const float m = m4[j];
      float r = r4[j];
      if (!isq) { float var = fmaxf(1.0f - 1e-5f * r * r, 0.f); r *= __builtin_amdgcn_rsqf(var + 1e-5f); }
      unsigned pk[5][2];
#pragma unroll
      for (int f = 0; f < 5; ++f) {
        f32x2 y0 = (lo2(acc[j][f]) - lo2(c4[f]) * m) * r, y1 = (hi2(acc[j][f]) - hi2(c4[f]) * m) * r;
        pk[f][0] = pack2v(isq ? phi2(y0) : gelu2(y0)); pk[f][1] = pack2v(isq ? phi2(y1) : gelu2(y1));
      }
      const unsigned orow = obase + tok * rstride;
      st16<ST_AUX>(rw, orow + pr0 + 16 * g, od ? u32x4{pk[1][0], pk[1][1], pk[0][0], pk[0][1]} : u32x4{pk[0][0], pk[0][1], pk[1][0], pk[1][1]});
      st16<ST_AUX>(rw, orow + pr1 + 16 * g, od ? u32x4{pk[3][0], pk[3][1], pk[2][0], pk[2][1]} : u32x4{pk[2][0], pk[2][1], pk[3][0], pk[3][1]});
      st8<ST_AUX>(rw, orow + 128 + 8 * g, u32x2{pk[4][0], pk[4][1]});
    }
    if (!isq && (panel == 0 || panel == 15) && wid == 0 && lane < 40)
      st16<ST_AUX>(rw, WS_HPAD + (panel == 0 ? 0 : 2050) * LD_A + (ct - 12) * 320 + (lane / 20) * LD_A + (lane % 20) * 16, u32x4{0, 0, 0, 0});
  }
}

extern "C" __global__ __launch_bounds__(512) void k1_kernel(K1Args a) {
  extern __shared__ __attribute__((aligned(16))) char smem[];
  LAS char* lds = (LAS char*)smem;
  const int wid = __builtin_amdgcn_readfirstlane(threadIdx.x >> 6), lane = threadIdx.x & 63;
  const int xcd = blockIdx.x & 7, j = blockIdx.x >> 3;
  const int panel = (xcd & 1) * 8 + (j & 7), ct = (xcd >> 1) * 4 + (j >> 3);
  const bool kv = (ct >= 4 && ct < 12);
  TS(a.ws, 0, 0);
  if (wid >= 4) {
    const int w = wid - 4;
    unsigned voffA[4], voffB[5];
#pragma unroll
    for (int i = 0; i < 4; ++i) {
      int row = (w + 4 * i) * 8 + (lane >> 3), pc = (lane & 7) ^ ((row >> 1) & 7);
      voffA[i] = (panel * 128 + row) * 1280 + pc * 16;
    }
    const char* bB; unsigned ldb;
    if (ct < 4) { bB = a.wqkv + ct * 320; ldb = 3840; }
    else if (ct < 12) { bB = a.wqkv + 1280 + (ct - 4) * 160; ldb = 3840; }
    else { bB = a.w1 + (ct - 12) * 320; ldb = 1280; }
#pragma unroll
    for (int i = 0; i < 5; ++i) {
      int P = (w + 4 * i) * 64 + lane, k = P / 20, qq = P - k * 20;
      unsigned cb;
      if (kv) { int sp = qq ^ (((k >> 3) & 1) << 1); int blk = sp / 5, r5 = sp - blk * 5; cb = (blk & 1) * 1280 + (blk >> 1) * 80 + r5 * 16; }
      else { int sp = qq ^ ((k >> 3) & 1); cb = sp * 16; }
      voffB[i] = k * ldb + cb;
    }
    const int rot = ROT1 ? (((j & 7) * 5) / 4 + ((j >> 3) * 5) / 2) % 10 : 0;
    const char* bA = a.x;
    auto issue = [&](int k, int slot) {
      int c = k + rot; if (c >= 10) c -= 10;
      const char* pa = pin_s(bA + c * 128);
      const char* pb = pin_s(bB + (size_t)c * 64 * ldb);
      LAS char* sb = lds + slot * K1_SLOT + w * 1024;
#pragma unroll
      for (int i = 0; i < 4; ++i) glds16(pa + voffA[i], sb + i * 4096);
#pragma unroll
      for (int i = 0; i < 5; ++i) glds16(pb + voffB[i], sb + K1_AB + i * 4096);
    };
    loader_loop<K1_NS, 10, 9, 2, 1, (USE_PF ? 1 : 0) + (PFC ? 1 : 0)>(issue, [&]() {
      {
        const char* cb = CODE_ADDR(k2_kernel);
        long len = CODE_ADDR(kz_end) - cb;
        long off = (long)(w * 64 + lane) * 128;
        if (off >= len) off = 0;
        __builtin_amdgcn_global_load_lds((GAS const void*)(cb + off), (LAS void*)(lds + K1_DUMMY), 4, 0, 0);
      }
      int id = blockIdx.x * 256 + w * 64 + lane;
      if (id >= 43520) id -= 43520;
      const char* p;
      if (id < 6400) p = a.w2 + (size_t)id * 128;
      else if (id < 12800) p = a.wo + (size_t)(id - 6400) * 128;
      else if (id < 28160) p = a.w3 + (size_t)(id - 12800) * 128;
      else p = a.w4 + (size_t)(id - 28160) * 128;
      __builtin_amdgcn_global_load_lds((GAS const void*)p, (LAS void*)(lds + K1_DUMMY), 4, 0, 0);
    });
    return;
  }
  prefetch_code(CODE_ADDR(k1_kernel), CODE_ADDR(k2_kernel), wid * 64 + lane, lds + K1_DUMMY);
  {
    int id = blockIdx.x * 256 + wid * 64 + lane;
    if (id >= 46080) id -= 46080;
    const char* p;
    if (id < 20480) p = a.x + (size_t)id * 128;
    else if (id < 39680) p = a.wqkv + (size_t)(id - 20480) * 128;
    else p = a.w1 + (size_t)(id - 39680) * 128;
    __builtin_amdgcn_global_load_lds((GAS const void*)p, (LAS void*)(lds + K1_DUMMY), 4, 0, 0);
  }
  if (kv) k1_consumer<1>(a, lds, wid, lane, panel, ct);
  else if (ct < 4) k1_consumer<0>(a, lds, wid, lane, panel, ct);
  else k1_consumer<2>(a, lds, wid, lane, panel, ct);
  TS(a.ws, 0, 3);
}

constexpr int T_NS = 7, T_AB = 96 * 128, T_BB = 64 * 128, T_SLOT = T_AB + T_BB;
constexpr int T_DUMMY = T_NS * T_SLOT;
constexpr int T_LDS = T_DUMMY + 256;

template <int NC>
DEVI void t96_consumer(LAS char* lds, int wid, int lane, f32x4 (&acc)[3][2], char* ws, int kk) {
  const int wr = wid >> 1, wc = wid & 1, g = lane >> 4, t = lane & 15, sw = t >> 1;
  unsigned aoff[2];
#pragma unroll
  for (int s = 0; s < 2; ++s) aoff[s] = (wr * 48 + t) * 128 + (((4 * s + g) ^ sw) * 16);
  const int q = t >> 2, p = lane & 3;
  const int swz = (((q >> 1) & 1) << 2) | (g & 1);
  const unsigned rowb = T_AB + (8 * g + q) * 128 + (((4 * wc + p) ^ swz) * 16);
  const unsigned bE = rowb + ((p & 1) ? 8 : 0), bO = rowb + ((p & 1) ? 0 : 8);
#pragma unroll
  for (int i = 0; i < 3; ++i) { acc[i][0] = f32x4{0.f, 0.f, 0.f, 0.f}; acc[i][1] = acc[i][0]; pin_v(acc[i][0]); pin_v(acc[i][1]); }
  bf16x8 X[2][2][3], W[2][2][2];
  auto ld = [&](auto SET, LAS const char* sb) {
    constexpr int st = decltype(SET)::value;
#pragma unroll
    for (int s = 0; s < 2; ++s) {
      X[st][s][0] = ld_frag(sb + aoff[s]);
      W[st][s][0] = ld_tr(sb + bE + s * 4096, sb + bE + s * 4096 + 512);
      W[st][s][1] = ld_tr(sb + bO + s * 4096, sb + bO + s * 4096 + 512);
      X[st][s][1] = ld_frag(sb + aoff[s] + 2048);
      X[st][s][2] = ld_frag(sb + aoff[s] + 4096);
    }
  };
  auto mm = [&](auto SET) {
    constexpr int st = decltype(SET)::value;
#pragma unroll
    for (int s = 0; s < 2; ++s)
#pragma unroll
      for (int i = 0; i < 3; ++i) { acc[i][0] = mfma(W[st][s][0], X[st][s][i], acc[i][0]); acc[i][1] = mfma(W[st][s][1], X[st][s][i], acc[i][1]); }
  };
  consumer_loop_full<NC, T_NS, T_SLOT, 12, 14>(lds, ld, mm, [&]() { TS(ws, kk, 1); });
  TS(ws, kk, 2);
}

template <int NC, bool SHIFT>
DEVI void t96_loader(LAS char* lds, int w, int lane, const char* A, unsigned lda, int row0, const char* B, unsigned ldb, int rot) {
  unsigned voffA[3], voffB[2];
  const int pslot = lane & 7;
  int pcs[3];
#pragma unroll
  for (int i = 0; i < 3; ++i) {
    int row = (w + 4 * i) * 8 + (lane >> 3), pc = pslot ^ ((row >> 1) & 7);
    int gr = row0 + row; if (gr > S_ - 1) gr = S_ - 1;
    voffA[i] = gr * lda + pc * 16; pcs[i] = pc * 8;
  }
#pragma unroll
  for (int i = 0; i < 2; ++i) {
    int k = (w + 4 * i) * 8 + (lane >> 3);
    int sp = pslot ^ ((((k >> 1) & 1) << 2) | ((k >> 3) & 1));
    voffB[i] = k * ldb + sp * 16;
  }
  auto issue = [&](int k, int slot) {
    int c = k + rot; if (c >= NC) c -= NC;
    const char* pa = pin_s(A + c * 128);
    const char* pb = pin_s(B + (size_t)c * 64 * ldb);
    LAS char* sb = lds + slot * T_SLOT + w * 1024;
#pragma unroll
    for (int i = 0; i < 3; ++i) {
      unsigned vo = voffA[i];
      if constexpr (SHIFT) {
        int kc = c * 64 + pcs[i];
        vo += (kc < 160) ? LD_A : (kc < 320) ? 3 * LD_A : (kc < 480) ? 0 : 4 * LD_A;
      }
      glds16(pa + vo, sb + i * 4096);
    }
#pragma unroll
    for (int i = 0; i < 2; ++i) glds16(pb + voffB[i], sb + T_AB + i * 4096);
  };
  loader_loop<T_NS, NC, 5, T_R, 2, 0>(issue, []() {});
}

DEVI void pair8(const f32x4& e, const f32x4& o, int g, f32x2 (&v)[4]) {
  const bool od = g & 1;
  v[0] = od ? lo2(o) : lo2(e); v[1] = od ? hi2(o) : hi2(e); v[2] = od ? lo2(e) : lo2(o); v[3] = od ? hi2(e) : hi2(o);
}
DEVI void t96_map(int tile, int& panel, int& ct, int& pin) {
  int grp = tile / 40, rem = tile - grp * 40;
  if (grp < 5) { ct = rem >> 2; pin = rem & 3; } else { ct = rem >> 1; pin = rem & 1; }
  panel = grp * 4 + pin;
}

struct K2Args { const char* x; const char* w2; char* ws; };

DEVI void k2_side(const K2Args& a, LAS char* lds, int wid, int lane, int hg, int tq) {
  const int tid = wid * 64 + lane;
  const char* src = a.ws + WS_PQ + ((size_t)hg * 2048 + tq * 512) * 160;
#pragma unroll
  for (int i = 0; i < 10; ++i) glds16(src + (size_t)((wid * 10 + i) * 1024 + lane * 16), lds + (wid * 10 + i) * 1024);
  LAS float* kvs = (LAS float*)(lds + 81920);
  LAS char* frg = lds + 81920 + 6720;
  if (tid < 420) {
    const f32x4* p = (const f32x4*)(a.ws + WS_KVP) + (size_t)hg * 420 + tid;
    f32x4 v[16];
#pragma unroll
    for (int i = 0; i < 16; ++i) v[i] = p[(size_t)i * 32 * 105];
#pragma unroll
    for (int i = 0; i < 8; ++i) v[i] += v[i + 8];
#pragma unroll
    for (int i = 0; i < 4; ++i) v[i] += v[i + 4];
    *(LAS f32x4*)(kvs + tid * 4) = (v[0] + v[1]) + (v[2] + v[3]);
  }
  LGKM0();
  BAR();
  const int g = lane >> 4, t = lane & 15;
  if (wid < 4) {
    LAS const float* kh = kvs + wid * 420;
    u32x4 u0, u1, uz;
#pragma unroll
    for (int e = 0; e < 4; ++e) {
      int d0 = 8 * g + 2 * e, d1 = d0 + 1;
      bool v = d0 < 20;
      int dd0 = v ? d0 : 0, dd1 = v ? d1 : 0;
      float a0 = kh[dd0 * 20 + t], a1 = kh[dd1 * 20 + t];
      float b0 = kh[dd0 * 20 + 16 + (t & 3)], b1 = kh[dd1 * 20 + 16 + (t & 3)];
      float z0 = kh[400 + dd0], z1 = kh[400 + dd1];
      u0[e] = v ? pack2(a0, a1) : 0u;
      u1[e] = (v && t < 4) ? pack2(b0, b1) : 0u;
      uz[e] = v ? pack2(z0, z1) : 0u;
    }
    *(LAS u32x4*)(frg + (wid * 3 + 0) * 1024 + lane * 16) = u0;
    *(LAS u32x4*)(frg + (wid * 3 + 1) * 1024 + lane * 16) = u1;
    *(LAS u32x4*)(frg + (wid * 3 + 2) * 1024 + lane * 16) = uz;
  }
  WAITV(0);
  LGKM0();
  BAR();
  bf16x8 A[4][3];
#pragma unroll
  for (int j = 0; j < 4; ++j)
#pragma unroll
    for (int k = 0; k < 3; ++k) A[j][k] = ld_frag(frg + (j * 3 + k) * 1024 + lane * 16);
#pragma unroll
  for (int i = 0; i < 4; ++i) {
    LAS char* row0 = lds + (wid * 64 + i * 16 + t) * 160;
    bf16x8 Bq[4];
#pragma unroll
    for (int j = 0; j < 4; ++j) {
      LAS char* row = row0 + 40 * j;
      u32x2 lo = *(LAS const u32x2*)(row + 16 * (g < 3 ? g : 0)), hi = *(LAS const u32x2*)(row + 16 * (g < 2 ? g : 0) + 8);
      u32x4 ub = {g < 3 ? lo[0] : 0u, g < 3 ? lo[1] : 0u, g < 2 ? hi[0] : 0u, g < 2 ? hi[1] : 0u};
      Bq[j] = __builtin_bit_cast(bf16x8, ub);
    }
    CBAR();
#pragma unroll
    for (int j = 0; j < 4; ++j) {
      LAS char* row = row0 + 40 * j;
      const f32x4 z4 = {0.f, 0.f, 0.f, 0.f};
      f32x4 d0 = mfma(A[j][0], Bq[j], z4), d1 = mfma(A[j][1], Bq[j], z4), dz = mfma(A[j][2], Bq[j], z4);
      float inv = __builtin_amdgcn_rcpf(dz[0] + 1e-6f);
      *(LAS u32x2*)(row + 8 * g) = u32x2{pack2v(lo2(d0) * inv), pack2v(hi2(d0) * inv)};
      if (g == 0) *(LAS u32x2*)(row + 32) = u32x2{pack2v(lo2(d1) * inv), pack2v(hi2(d1) * inv)};
    }
  }
  const rsrc_t rw = mk_rsrc(a.ws, WS_END);
  const unsigned dst = WS_O + (tq * 512 + wid * 64) * LD_A + hg * 160;
  LGKM0();
#pragma unroll
  for (int i = 0; i < 10; ++i) {
    int P = i * 64 + lane, r = P / 10, qq = P - r * 10;
    u32x4 v = *(LAS const u32x4*)(lds + (wid * 64) * 160 + P * 16);
    st16<ST_AUX>(rw, dst + r * LD_A + qq * 16, v);
  }
}

extern "C" __global__ __launch_bounds__(512) void k2_kernel(K2Args a) {
  extern __shared__ __attribute__((aligned(16))) char smem[];
  LAS char* lds = (LAS char*)smem;
  const int wid = __builtin_amdgcn_readfirstlane(threadIdx.x >> 6), lane = threadIdx.x & 63;
  const int xcd = blockIdx.x & 7, j = blockIdx.x >> 3;
  TS(a.ws, 1, 0);
  if (wid < 4) prefetch_code(CODE_ADDR(k2_kernel), CODE_ADDR(k3_kernel), wid * 64 + lane, lds + T_DUMMY);
  if (j >= 28) { int sidx = (j - 28) * 8 + xcd; k2_side(a, lds, wid, lane, sidx & 7, sidx >> 3); TS(a.ws, 1, 3); return; }
  const int tile = xcd * 28 + j;
  if (tile >= 220) return;
  int panel, ct, pin;
  t96_map(tile, panel, ct, pin);
  if (wid >= 4) {
    t96_loader<10, true>(lds, wid - 4, lane, a.ws + WS_HPAD, LD_A, panel * 96, a.w2 + ct * 128, 1280, USE_ROT ? (ct + (pin * 5) / 2) % 10 : 0);
    return;
  }
  const int wr = wid >> 1, wc = wid & 1, g = lane >> 4, t = lane & 15;
  const rsrc_t rx = mk_rsrc(a.x, S_ * 1280), rw = mk_rsrc(a.ws, WS_END);
  u32x4 xv[3]; f32x2 st[3]; int tok[3];
#pragma unroll
  for (int i = 0; i < 3; ++i) {
    tok[i] = panel * 96 + wr * 48 + i * 16 + t;
    int tk = tok[i] < S_ ? tok[i] : S_ - 1;
    xv[i] = ld16(rx, tk * 1280 + ct * 128 + wc * 64 + g * 16);
    st[i] = ld8f(rw, WS_XST + tk * 8);
  }
  f32x4 acc[3][2];
  t96_consumer<10>(lds, wid, lane, acc, a.ws, 1);
#pragma unroll
  for (int i = 0; i < 3; ++i) {
    f32x2 v[4];
    pair8(acc[i][0], acc[i][1], g, v);
    u32x4 o; float s1 = 0.f, s2 = 0.f;
#pragma unroll
    for (int e = 0; e < 4; ++e) {
      unsigned xn = pack2v((unpack2(xv[i][e]) - st[i][0]) * st[i][1]);
      o[e] = pack2v(unpack2(xn) + v[e]);
      s1 = dot2acc(o[e], 0x3f803f80u, s1); s2 = dot2acc(o[e], o[e], s2);
    }
    s1 = red_g(s1); s2 = red_g(s2);
    if (tok[i] < S_) {
      st16<ST_AUX>(rw, WS_X1 + tok[i] * LD_A + ct * 128 + wc * 64 + g * 16, o);
      if (g == 0) st8f<ST_AUX>(rw, WS_X1P + ((2 * ct + wc) * 2048 + tok[i]) * 8, f32x2{s1, s2});
    }
  }
  TS(a.ws, 1, 3);
}

constexpr int K3_NS = 4, K3_AB = 144 * 128, K3_BB = 64 * 256, K3_SLOT = K3_AB + K3_BB;
constexpr int K3_TAB = K3_NS * K3_SLOT;
constexpr int K3_DUMMY = K3_TAB + 144 * 8;
constexpr int K3_LDS = K3_DUMMY + 256;

struct K3Args { const char* w3; const char* wo; char* ws; };

template <bool MLP>
DEVI void k3_consumer(const K3Args& a, LAS char* lds, int wid, int lane, int panel, int ct) {
  const int g = lane >> 4, t = lane & 15, sw = t >> 1;
  LAS f32x2* stat = (LAS f32x2*)(lds + K3_TAB);
  const rsrc_t rw = mk_rsrc(a.ws, WS_END);
  f32x2 part[20];
  const int tid = wid * 64 + lane;
  if constexpr (MLP) {
    int tk = panel * 144 + (tid < 144 ? tid : 0); if (tk > S_ - 1) tk = S_ - 1;
    if (wid < 3) {
#pragma unroll
      for (int i = 0; i < 20; ++i) part[i] = ld8f(rw, WS_X1P + (i * 2048 + tk) * 8);
    }
  }
  unsigned aoff[2];
#pragma unroll
  for (int s = 0; s < 2; ++s) aoff[s] = t * 128 + (((4 * s + g) ^ sw) * 16);
  const int q = t >> 2, p = lane & 3;
  const int swz = (q << 2) | (g & 1);
  const unsigned rowb = K3_AB + (8 * g + q) * 256 + (((4 * wid + p) ^ swz) * 16);
  const unsigned bE = rowb + ((p & 1) ? 8 : 0), bO = rowb + ((p & 1) ? 0 : 8);
  f32x4 acc[9][2];
#pragma unroll
  for (int i = 0; i < 9; ++i) { acc[i][0] = f32x4{0.f, 0.f, 0.f, 0.f}; acc[i][1] = acc[i][0]; pin_v(acc[i][0]); pin_v(acc[i][1]); }
  f32x4 cE = {0.f, 0.f, 0.f, 0.f}, cO = cE;
  bf16x8 ones8 = __builtin_bit_cast(bf16x8, u32x4{0x3f803f80u, 0x3f803f80u, 0x3f803f80u, 0x3f803f80u});
  if constexpr (MLP) { pin_v(cE); pin_v(cO); pin_v(ones8); }
  bf16x8 X[2][9], W[2][2];
  auto ld = [&](auto SET, LAS const char* sb, auto SS) {
    constexpr int st = decltype(SET)::value, s = decltype(SS)::value;
    X[st][0] = ld_frag(sb + aoff[s]);
    W[st][0] = ld_tr(sb + bE + s * 8192, sb + bE + s * 8192 + 1024);
    W[st][1] = ld_tr(sb + bO + s * 8192, sb + bO + s * 8192 + 1024);
#pragma unroll
    for (int j = 1; j < 9; ++j) X[st][j] = ld_frag(sb + aoff[s] + j * 2048);
  };
  auto mm = [&](auto SET) {
    constexpr int st = decltype(SET)::value;
#pragma unroll
    for (int i = 0; i < 9; ++i) { acc[i][0] = mfma(W[st][0], X[st][i], acc[i][0]); acc[i][1] = mfma(W[st][1], X[st][i], acc[i][1]); }
    if constexpr (MLP) { cE = mfma(W[st][0], ones8, cE); cO = mfma(W[st][1], ones8, cO); }
  };
  auto pre = [&]() {
    TS(a.ws, 2, 1);
    if constexpr (MLP) {
      if (wid < 3) {
#pragma unroll
        for (int i = 0; i < 10; ++i) part[i] += part[i + 10];
#pragma unroll
        for (int i = 0; i < 5; ++i) part[i] += part[i + 5];
        f32x2 s = (part[0] + part[1]) + (part[2] + part[3]) + part[4];
        float m = s[0] * (1.0f / 640.0f);
        float var = fmaxf(s[1] * (1.0f / 640.0f) - m * m, 0.f);
        if (tid < 144) stat[tid] = f32x2{m, __builtin_amdgcn_rsqf(var + 1e-5f)};
      }
    }
  };
  consumer_loop<10, K3_NS, K3_SLOT, MLP ? 20 : 18, 13, 0>(lds, ld, mm, pre);
  TS(a.ws, 2, 2);
  const unsigned obase = MLP ? WS_HID + ct * 256 + wid * 64 + g * 16 : WS_S + (ct - 12) * 256 + wid * 64 + g * 16;
  const unsigned rs = MLP ? LD_HID : 1280;
  const bool od = g & 1;
#pragma unroll
  for (int i = 0; i < 9; ++i) {
    const int tok = panel * 144 + i * 16 + t;
    f32x2 e0 = lo2(acc[i][0]), e1 = hi2(acc[i][0]), o0 = lo2(acc[i][1]), o1 = hi2(acc[i][1]);
    if constexpr (MLP) {
      const f32x2 st = stat[i * 16 + t];
      e0 = gelu2((e0 - lo2(cE) * st[0]) * st[1]); e1 = gelu2((e1 - hi2(cE) * st[0]) * st[1]);
      o0 = gelu2((o0 - lo2(cO) * st[0]) * st[1]); o1 = gelu2((o1 - hi2(cO) * st[0]) * st[1]);
    }
    unsigned pe0 = pack2v(e0), pe1 = pack2v(e1), po0 = pack2v(o0), po1 = pack2v(o1);
    if (tok < S_) st16<ST_AUX>(rw, obase + tok * rs, od ? u32x4{po0, po1, pe0, pe1} : u32x4{pe0, pe1, po0, po1});
  }
}

extern "C" __global__ __launch_bounds__(512) void k3_kernel(K3Args a) {
  extern __shared__ __attribute__((aligned(16))) char smem[];
  LAS char* lds = (LAS char*)smem;
  const int wid = __builtin_amdgcn_readfirstlane(threadIdx.x >> 6), lane = threadIdx.x & 63;
  const int xcd = blockIdx.x & 7, j = blockIdx.x >> 3;
  const int tile = xcd * 32 + j;
  if (tile >= 255) return;
  const int grp = tile / 68, rem = tile - grp * 68;
  int ct, pin;
  if (grp < 3) { ct = rem >> 2; pin = rem & 3; } else { ct = rem / 3; pin = rem - ct * 3; }
  const int panel = grp * 4 + pin;
  const bool mlp = ct < 12;
  TS(a.ws, 2, 0);
  if (wid >= 4) {
    const int w = wid - 4;
    const char* A = mlp ? a.ws + WS_X1 : a.ws + WS_O;
    const char* B = mlp ? a.w3 + ct * 256 : a.wo + (ct - 12) * 256;
    const unsigned ldb = mlp ? 3072 : 1280;
    unsigned voffA[5], voffB[4];
#pragma unroll
    for (int i = 0; i < 5; ++i) {
      int ii = w + 4 * i; if (ii > 17) ii = 17;
      int row = ii * 8 + (lane >> 3), pc = (lane & 7) ^ ((row >> 1) & 7);
      int gr = panel * 144 + row; if (gr > S_ - 1) gr = S_ - 1;
      voffA[i] = gr * LD_A + pc * 16;
    }
#pragma unroll
    for (int i = 0; i < 4; ++i) {
      int P = (w + 4 * i) * 64 + lane, k = P >> 4, qq = P & 15;
      int sp = qq ^ (((k & 3) << 2) | ((k >> 3) & 1));
      voffB[i] = k * ldb + sp * 16;
    }
    const int last = (w + 16 > 17) ? 17 : w + 16;
    const int rot = USE_ROT ? ((pin * 5) / 2 + ((ct & 7) * 5) / 4) % 10 : 0;
    auto issue = [&](int k, int slot) {
      int c = k + rot; if (c >= 10) c -= 10;
      const char* pa = pin_s(A + c * 128);
      const char* pb = pin_s(B + (size_t)c * 64 * ldb);
      LAS char* sb = lds + slot * K3_SLOT;
#pragma unroll
      for (int i = 0; i < 4; ++i) glds16(pa + voffA[i], sb + (w + 4 * i) * 1024);
      glds16(pa + voffA[4], sb + last * 1024);
#pragma unroll
      for (int i = 0; i < 4; ++i) glds16(pb + voffB[i], sb + K3_AB + (w + 4 * i) * 1024);
    };
    loader_loop<K3_NS, 10, 9, 2, 1, 0>(issue, []() {});
    return;
  }
  prefetch_code(CODE_ADDR(k3_kernel), CODE_ADDR(k4_kernel), wid * 64 + lane, lds + K3_DUMMY);
  if (mlp) k3_consumer<true>(a, lds, wid, lane, panel, ct);
  else k3_consumer<false>(a, lds, wid, lane, panel, ct);
  TS(a.ws, 2, 3);
}

struct K4Args { const char* x; const char* w4; char* ws; char* out; };

extern "C" __global__ __launch_bounds__(512) void k4_kernel(K4Args a) {
  extern __shared__ __attribute__((aligned(16))) char smem[];
  LAS char* lds = (LAS char*)smem;
  const int wid = __builtin_amdgcn_readfirstlane(threadIdx.x >> 6), lane = threadIdx.x & 63;
  const int xcd = blockIdx.x & 7, j = blockIdx.x >> 3;
  const int tile = xcd * 28 + j;
  if (j >= 28 || tile >= 220) return;
  int panel, ct, pin;
  t96_map(tile, panel, ct, pin);
  TS(a.ws, 3, 0);
  if (wid >= 4) {
    t96_loader<24, false>(lds, wid - 4, lane, a.ws + WS_HID, LD_HID, panel * 96, a.w4 + ct * 128, 1280, USE_ROT ? (pin * 6 + (ct * 24) / 7) % 24 : 0);
    return;
  }
  prefetch_code(CODE_ADDR(k4_kernel), CODE_ADDR(kz_end), wid * 64 + lane, lds + T_DUMMY);
  const int wr = wid >> 1, wc = wid & 1, g = lane >> 4, t = lane & 15;
  const rsrc_t rx = mk_rsrc(a.x, S_ * 1280), rw = mk_rsrc(a.ws, WS_END), ro = mk_rsrc(a.out, S_ * 1280);
  u32x4 xv[3], x1v[3], sv[3]; int tok[3];
#pragma unroll
  for (int i = 0; i < 3; ++i) {
    tok[i] = panel * 96 + wr * 48 + i * 16 + t;
    int tk = tok[i] < S_ ? tok[i] : S_ - 1;
    unsigned off = tk * 1280 + ct * 128 + wc * 64 + g * 16;
    xv[i] = ld16(rx, off);
    x1v[i] = ld16(rw, WS_X1 + tk * LD_A + ct * 128 + wc * 64 + g * 16);
    sv[i] = ld16(rw, WS_S + off);
  }
  f32x4 acc[3][2];
  t96_consumer<24>(lds, wid, lane, acc, a.ws, 3);
#pragma unroll
  for (int i = 0; i < 3; ++i) {
    f32x2 v[4];
    pair8(acc[i][0], acc[i][1], g, v);
    u32x4 o;
#pragma unroll
    for (int e = 0; e < 4; ++e) o[e] = pack2v(unpack2(xv[i][e]) + unpack2(sv[i][e]) * silu2(unpack2(x1v[i][e]) + v[e]));
    st16<OUT_AUX>(ro, tok[i] * 1280 + ct * 128 + wc * 64 + g * 16, o);
  }
  TS(a.ws, 3, 3);
}

extern "C" __global__ void kz_end() {}

extern "C" void kernel_launch(void* const* d_in, const int* in_sizes, int n_in, void* d_out, int out_size, void* d_ws, size_t ws_size,
                              hipStream_t stream) {
  const char* x = (const char*)d_in[0];
  const char* wqkv = (const char*)d_in[1];
  const char* wo = (const char*)d_in[2];
  const char* w1 = (const char*)d_in[4];
  const char* w2 = (const char*)d_in[5];
  const char* w3 = (const char*)d_in[6];
  const char* w4 = (const char*)d_in[7];
  char* ws = (char*)d_ws;
  (void)hipFuncSetAttribute((const void*)k1_kernel, hipFuncAttributeMaxDynamicSharedMemorySize, K1_LDS);
  (void)hipFuncSetAttribute((const void*)k2_kernel, hipFuncAttributeMaxDynamicSharedMemorySize, T_LDS);
  (void)hipFuncSetAttribute((const void*)k3_kernel, hipFuncAttributeMaxDynamicSharedMemorySize, K3_LDS);
  (void)hipFuncSetAttribute((const void*)k4_kernel, hipFuncAttributeMaxDynamicSharedMemorySize, T_LDS);
  k1_kernel<<<256, 512, K1_LDS, stream>>>(K1Args{x, wqkv, w1, w2, w3, w4, wo, ws});
  k2_kernel<<<256, 512, T_LDS, stream>>>(K2Args{x, w2, ws});
  k3_kernel<<<256, 512, K3_LDS, stream>>>(K3Args{w3, wo, ws});
  k4_kernel<<<256, 512, T_LDS, stream>>>(K4Args{x, w4, ws, (char*)d_out});
}
```

```cpp
#include <hip/hip_runtime.h>
#include <stdint.h>

#define LAS __attribute__((address_space(3)))
#define GAS __attribute__((address_space(1)))
#define DEVI __device__ __forceinline__

typedef __bf16 bf16x2 __attribute__((ext_vector_type(2)));
typedef __bf16 bf16x8 __attribute__((ext_vector_type(8)));
typedef float f32x2 __attribute__((ext_vector_type(2)));
typedef float f32x4 __attribute__((ext_vector_type(4)));
typedef short s16x4 __attribute__((ext_vector_type(4)));
typedef unsigned u32x2 __attribute__((ext_vector_type(2)));
typedef unsigned u32x4 __attribute__((ext_vector_type(4)));
typedef __amdgpu_buffer_rsrc_t rsrc_t;

#define ST_AUX 16
#define OUT_AUX 0
#define USE_ROT 0
#define USE_PF 1

#define T_R 4
#define ROT1 0
#define PF1 1
constexpr int S_ = 2048;

#define LD_A 1408
#define LD_HID 3200
constexpr unsigned WS_HPAD = 0;
constexpr unsigned WS_PQ   = WS_HPAD + 2052 * LD_A;
constexpr unsigned WS_KVP  = WS_PQ + 2048 * 640 * 2;
constexpr unsigned WS_XST  = WS_KVP + 16 * 32 * 420 * 4;
constexpr unsigned WS_X1   = WS_XST + 2048 * 8;
constexpr unsigned WS_X1P  = WS_X1 + 2048 * LD_A;
constexpr unsigned WS_O    = WS_X1P + 20 * 2048 * 8;
constexpr unsigned WS_HID  = WS_O + 2048 * LD_A;
constexpr unsigned WS_S    = WS_HID + 2048 * LD_HID;
constexpr unsigned WS_END  = WS_S + 2048 * 640 * 2;

template <int V> struct IC { static constexpr int value = V; };
DEVI unsigned pack2(float a, float b) { f32x2 v = {a, b}; return __builtin_bit_cast(unsigned, __builtin_convertvector(v, bf16x2)); }
DEVI unsigned pack2v(f32x2 v) { return __builtin_bit_cast(unsigned, __builtin_convertvector(v, bf16x2)); }
DEVI float bflo(unsigned u) { return __builtin_bit_cast(float, u << 16); }
DEVI float bfhi(unsigned u) { return __builtin_bit_cast(float, u & 0xffff0000u); }
DEVI f32x2 unpack2(unsigned u) { return f32x2{bflo(u), bfhi(u)}; }
DEVI float dot2acc(unsigned a, unsigned b, float c) { return __builtin_amdgcn_fdot2_f32_bf16(__builtin_bit_cast(bf16x2, a), __builtin_bit_cast(bf16x2, b), c, false); }
DEVI f32x2 lo2(f32x4 v) { return f32x2{v[0], v[1]}; }
DEVI f32x2 hi2(f32x4 v) { return f32x2{v[2], v[3]}; }
DEVI f32x2 gelu2(f32x2 u) {
  const float c1 = -2.3022081986f, c2 = -0.10294324f;
  f32x2 w = u * (u * u * c2 + c1);
  f32x2 d = f32x2{__builtin_amdgcn_exp2f(w[0]), __builtin_amdgcn_exp2f(w[1])} + 1.0f;
  return u * f32x2{__builtin_amdgcn_rcpf(d[0]), __builtin_amdgcn_rcpf(d[1])};
}
DEVI f32x2 silu2(f32x2 b) {
  f32x2 w = b * -1.4426950409f;
  f32x2 d = f32x2{__builtin_amdgcn_exp2f(w[0]), __builtin_amdgcn_exp2f(w[1])} + 1.0f;
  return b * f32x2{__builtin_amdgcn_rcpf(d[0]), __builtin_amdgcn_rcpf(d[1])};
}
DEVI f32x2 phi2(f32x2 t) {
  f32x2 w = t * 1.4426950409f;
  f32x2 e = {__builtin_amdgcn_exp2f(fminf(w[0], 0.f)), __builtin_amdgcn_exp2f(fminf(w[1], 0.f))};
  return (f32x2{fmaxf(t[0], 0.f), fmaxf(t[1], 0.f)} + e) + 1e-6f;
}
DEVI float red_g(float v) { v += __shfl_xor(v, 16); v += __shfl_xor(v, 32); return v; }
DEVI f32x4 mfma(bf16x8 a, bf16x8 b, f32x4 c) { return __builtin_amdgcn_mfma_f32_16x16x32_bf16(a, b, c, 0, 0, 0); }
DEVI bf16x8 ld_frag(LAS const char* p) { return *(LAS const bf16x8*)p; }
DEVI bf16x8 ld_tr(LAS const char* p0, LAS const char* p1) {
  s16x4 a = __builtin_amdgcn_ds_read_tr16_b64_v4i16((LAS s16x4*)p0);
  s16x4 b = __builtin_amdgcn_ds_read_tr16_b64_v4i16((LAS s16x4*)p1);
  return __builtin_bit_cast(bf16x8, __builtin_shufflevector(a, b, 0, 1, 2, 3, 4, 5, 6, 7));
}
DEVI void glds16(const char* g, LAS char* l) { __builtin_amdgcn_global_load_lds((GAS const void*)g, (LAS void*)l, 16, 0, 0); }
DEVI rsrc_t mk_rsrc(const void* p, unsigned bytes) { return __builtin_amdgcn_make_buffer_rsrc((void*)p, 0, (int)bytes, 0x00020000); }
template <int AUX> DEVI void st16(rsrc_t r, unsigned off, u32x4 v) { __builtin_amdgcn_raw_buffer_store_b128(v, r, off, 0, AUX); }
template <int AUX> DEVI void st8(rsrc_t r, unsigned off, u32x2 v) { __builtin_amdgcn_raw_buffer_store_b64(v, r, off, 0, AUX); }
template <int AUX> DEVI void st8f(rsrc_t r, unsigned off, f32x2 v) { __builtin_amdgcn_raw_buffer_store_b64(__builtin_bit_cast(u32x2, v), r, off, 0, AUX); }
template <int AUX> DEVI void st4f(rsrc_t r, unsigned off, float v) { __builtin_amdgcn_raw_buffer_store_b32(__builtin_bit_cast(unsigned, v), r, off, 0, AUX); }
DEVI u32x4 ld16(rsrc_t r, unsigned off) { return __builtin_amdgcn_raw_buffer_load_b128(r, off, 0, 0); }
DEVI f32x2 ld8f(rsrc_t r, unsigned off) { return __builtin_bit_cast(f32x2, __builtin_amdgcn_raw_buffer_load_b64(r, off, 0, 0)); }
#define CBAR() asm volatile("" ::: "memory")
#define BAR() do { CBAR(); __builtin_amdgcn_s_barrier(); CBAR(); } while (0)
#define SB0() __builtin_amdgcn_sched_barrier(0)
#define LGKM0() __builtin_amdgcn_s_waitcnt(0xC07F)
#define WAITV(n) asm volatile("s_waitcnt vmcnt(%0)" ::"i"(n) : "memory")
DEVI const char* pin_s(const char* p) { asm volatile("" : "+s"(p)); return p; }
template <class T> DEVI void pin_v(T& v) { asm volatile("" : "+v"(v)); }

#define PFC 1
#define CODE_ADDR(sym) ({ unsigned lo_, hi_; asm volatile("s_getpc_b64 s[40:41]\n\ts_add_u32 s40, s40, " #sym "@rel32@lo+4\n\ts_addc_u32 s41, s41, " #sym "@rel32@hi+12\n\ts_mov_b32 %0, s40\n\ts_mov_b32 %1, s41" : "=s"(lo_), "=s"(hi_) : : "s40", "s41", "scc"); (const char*)(((unsigned long long)hi_ << 32) | lo_); })
DEVI unsigned touch(const char* p) { unsigned d; asm volatile("global_load_dword %0, %1, off" : "=v"(d) : "v"(p)); return d; }
DEVI void touch_done(unsigned d) { asm volatile("s_waitcnt vmcnt(0)" : : "v"(d)); }
DEVI unsigned prefetch_code(const char* beg, const char* end, int idx) {
  long len = end - beg, off = (long)idx * 128;
  return touch(beg + (off < len ? off : 0));
}

#define TS(ws, k, j) do {} while (0)

template <int NS, int NC, int NI, int R, int LAG, int XI, class Issue, class Hook>
DEVI void loader_loop(Issue&& issue_, Hook&& hook) {
  auto& issue = issue_;
#pragma unroll
  for (int k = 0; k < R; ++k) issue(k, k);
  WAITV((R - 1) * NI);
  BAR();
#pragma unroll
  for (int k = R; k < NS; ++k) issue(k, k);
#pragma unroll
  for (int k = 1; k < LAG; ++k) { WAITV((NS - 1 - k) * NI); BAR(); }
  int slot = 0;
#pragma unroll 1
  for (int k = LAG; k <= NC - NS + LAG - 1; ++k) {
    WAITV((NS - LAG - 1) * NI);
    BAR();
    issue(k - LAG + NS, slot);
    slot = (slot + 1 == NS) ? 0 : slot + 1;
  }
  hook();
#pragma unroll
  for (int k = NC - NS + LAG; k < NC; ++k) {
    WAITV((NC - k - 1) * NI + XI);
    BAR();
  }
}

template <int NM, int ND, int NV>
DEVI void sched_region() {
  constexpr int DPER = (ND + NM - 3) / (NM - 2);
  constexpr int VPER = (NV + NM - 1) / NM;
#pragma unroll
  for (int i = 0; i < NM; ++i) {
    __builtin_amdgcn_sched_group_barrier(0x008, 1, 0);
    if (i * DPER < ND) {
      __builtin_amdgcn_sched_group_barrier(0x002, 1, 0);
      __builtin_amdgcn_sched_group_barrier(0x100, DPER, 0);
    }
    if (VPER > 0) __builtin_amdgcn_sched_group_barrier(0x002, VPER, 0);
    __builtin_amdgcn_sched_group_barrier(0x004, 1, 0);
  }
}

template <int NC, int NS, int SLOT, int NM, int ND, int NV, class LD, class MM, class PRE>
DEVI void consumer_loop(LAS const char* lds, LD&& ld_, MM&& mm_, PRE&& pre) {
  auto& ld = ld_; auto& mm = mm_;
  BAR();
  ld(IC<0>{}, lds, IC<0>{});
  SB0();
  pre();
  SB0();
  LAS const char* sb = lds;
#pragma unroll 1
  for (int c = 0; c < NC - 1; ++c) {
    ld(IC<1>{}, sb, IC<1>{});
    mm(IC<0>{});
    sb = (sb == lds + (NS - 1) * SLOT) ? lds : sb + SLOT;
    sched_region<NM, ND, NV>();
    SB0();
    LGKM0();
    BAR();
    SB0();
    ld(IC<0>{}, sb, IC<0>{});
    mm(IC<1>{});
    sched_region<NM, ND, NV>();
    SB0();
  }
  ld(IC<1>{}, sb, IC<1>{});
  mm(IC<0>{});
  sched_region<NM, ND, NV>();
  SB0();
  mm(IC<1>{});
}

template <int NC, int NS, int SLOT, int NM, int ND, class LD, class MM, class PRE>
DEVI void consumer_loop_full(LAS const char* lds, LD&& ld, MM&& mm, PRE&& pre) {
  BAR();
  ld(IC<0>{}, lds);
  SB0();
  pre();
  SB0();
  LAS const char* s1 = lds + SLOT;
#pragma unroll 1
  for (int c = 0; c + 2 < NC; c += 2) {
    LAS const char* s2 = (s1 == lds + (NS - 1) * SLOT) ? lds : s1 + SLOT;
    BAR();
    SB0();
    ld(IC<1>{}, s1);
    mm(IC<0>{});
    s1 = (s2 == lds + (NS - 1) * SLOT) ? lds : s2 + SLOT;
    sched_region<NM, ND, 0>();
    SB0();
    BAR();
    SB0();
    ld(IC<0>{}, s2);
    mm(IC<1>{});
    sched_region<NM, ND, 0>();
    SB0();
  }
  BAR();
  SB0();
  ld(IC<1>{}, s1);
  mm(IC<0>{});
  sched_region<NM, ND, 0>();
  SB0();
  mm(IC<1>{});
}

constexpr int K1_BM = 128, K1_BN = 160, K1_NS = 4;
constexpr int K1_AB = K1_BM * 128, K1_BB = 64 * K1_BN * 2, K1_SLOT = K1_AB + K1_BB;
constexpr int K1_TAB = K1_NS * K1_SLOT;
constexpr int K1_DUMMY = K1_TAB + 128 * 8 + 160 * 4;
constexpr int K1_LDS = K1_DUMMY + 256;

struct K1Args { const char* x; const char* wqkv; const char* w1; const char* w2; const char* w3; const char* w4; const char* wo; char* ws; };

template <int MODE>
DEVI void k1_consumer(const K1Args& a, LAS char* lds, int wid, int lane, int panel, int ct, const unsigned (&tc)[2]) {
  constexpr bool KV = (MODE == 1);
  const int wr = wid >> 1, wc = wid & 1, g = lane >> 4, t = lane & 15;
  const int sw = t >> 1;
  unsigned aoff[2][2];
#pragma unroll
  for (int s = 0; s < 2; ++s)
#pragma unroll
    for (int hf = 0; hf < 2; ++hf) aoff[s][hf] = (wr * 64 + ((2 * hf + 2 * wc) & 3) * 16 + t) * 128 + (((4 * s + g) ^ sw) * 16);
  const int q = t >> 2, p = lane & 3;
  unsigned boff[5];
  if constexpr (KV) {
    const unsigned rowb = (8 * g + q) * 320 + 8 * p;
    const int odd = g & 1;
    boff[0] = K1_AB + rowb + 160 * wc + (odd ? ((wc & 1) ? -32 : 32) : 0);
    boff[1] = K1_AB + rowb + 160 * wc + (odd ? ((wc & 1) ? 32 : -32) : 0);
    boff[2] = boff[3] = boff[4] = 0;
  } else {
    const unsigned rowb = K1_AB + (8 * g + q) * 320 + 160 * wc;
    const int pp = p ^ (g & 1);
    const int pr0 = wr ? 64 : 0, pr1 = wr ? 0 : 64;
    const unsigned bE = rowb + pp * 16 + ((p & 1) ? 8 : 0), bO = rowb + pp * 16 + ((p & 1) ? 0 : 8);
    boff[0] = bE + pr0; boff[1] = bO + pr0; boff[2] = bE + pr1; boff[3] = bO + pr1;
    boff[4] = rowb + 128 + (((p >> 1) ^ (g & 1)) * 16) + (p & 1) * 8;
  }
  unsigned el[4][2];
  if constexpr (KV) {
#pragma unroll
    for (int i = 0; i < 4; ++i)
#pragma unroll
      for (int u = 0; u < 2; ++u) {
        int idx = (wid * 64 + lane + 256 * i) * 2 + u; if (idx >= 1680) idx = 0;
        int hl = idx / 420, rem = idx - hl * 420, w2 = hl >> 1, hb = hl & 1;
        int dd, k, col;
        if (rem < 400) {
          dd = rem / 20; int e = rem - dd * 20;
          int kc = 20 * hb + dd, vc = 40 + 20 * hb + e;
          int fa = kc >> 4, fb = vc >> 4;
          k = (fa == 0) ? fb - 2 : (fa == 1) ? fb : fb + 2;
          col = vc & 15; dd = kc & 15;
        } else {
          int kc = 20 * hb + (rem - 400);
          k = 7 + (kc >> 4); col = 0; dd = kc & 15;
        }
        el[i][u] = (((k * 64 + (dd >> 2) * 16 + col) * 4 + (dd & 3)) + (w2 & 1) * 2560) * 4;
        pin_v(el[i][u]);
      }
  }

  f32x4 acc[4][5];
#pragma unroll
  for (int i = 0; i < 4; ++i)
#pragma unroll
    for (int f = 0; f < 5; ++f) { acc[i][f] = f32x4{0.f, 0.f, 0.f, 0.f}; pin_v(acc[i][f]); }
  f32x4 s1[2], s2[2], cs[3];
#pragma unroll
  for (int j = 0; j < 2; ++j) { s1[j] = f32x4{0.f, 0.f, 0.f, 0.f}; s2[j] = s1[j]; pin_v(s1[j]); pin_v(s2[j]); }
#pragma unroll
  for (int j = 0; j < 3; ++j) { cs[j] = f32x4{0.f, 0.f, 0.f, 0.f}; pin_v(cs[j]); }
  const unsigned ONES = 0x3f803f80u;
  bf16x8 ones8 = __builtin_bit_cast(bf16x8, u32x4{ONES, ONES, ONES, ONES});
  pin_v(ones8);

  bf16x8 X[2][4], W[2][5];
  auto ld = [&](auto SET, LAS const char* sb, auto SS) {
    constexpr int st = decltype(SET)::value, s = decltype(SS)::value;
    if constexpr (KV) {
      X[st][0] = ld_frag(sb + aoff[s][0]);
      { LAS const char* pb = sb + boff[0] + s * (32 * 320); W[st][0] = ld_tr(pb, pb + 4 * 320); }
      { LAS const char* pb = sb + boff[1] + 32 + s * (32 * 320); W[st][1] = ld_tr(pb, pb + 4 * 320); }
      { LAS const char* pb = sb + boff[0] + 64 + s * (32 * 320); W[st][2] = ld_tr(pb, pb + 4 * 320); }
      { LAS const char* pb = sb + boff[1] + 96 + s * (32 * 320); W[st][3] = ld_tr(pb, pb + 4 * 320); }
      { LAS const char* pb = sb + boff[0] + 128 + s * (32 * 320); W[st][4] = ld_tr(pb, pb + 4 * 320); }
      X[st][1] = ld_frag(sb + aoff[s][0] + 2048);
      X[st][2] = ld_frag(sb + aoff[s][1]);
      X[st][3] = ld_frag(sb + aoff[s][1] + 2048);
    } else {
      X[st][0] = ld_frag(sb + aoff[s][0]);
#pragma unroll
      for (int f = 0; f < 5; ++f) { LAS const char* pb = sb + boff[f] + s * (32 * 320); W[st][f] = ld_tr(pb, pb + 4 * 320); }
      X[st][1] = ld_frag(sb + aoff[s][0] + 2048);
      X[st][2] = ld_frag(sb + aoff[s][1]);
      X[st][3] = ld_frag(sb + aoff[s][1] + 2048);
    }
  };
  auto mm = [&](auto SET) {
    constexpr int st = decltype(SET)::value;
#pragma unroll
    for (int i = 0; i < 4; ++i)
#pragma unroll
      for (int f = 0; f < 5; ++f) acc[i][f] = KV ? mfma(X[st][i], W[st][f], acc[i][f]) : mfma(W[st][f], X[st][i], acc[i][f]);
#pragma unroll
    for (int j = 0; j < 2; ++j) { s1[j] = mfma(ones8, X[st][j], s1[j]); s2[j] = mfma(X[st][j], X[st][j], s2[j]); }
#pragma unroll
    for (int j = 0; j < 3; ++j) {
      if constexpr (KV) cs[j] = mfma(ones8, wr ? W[st][j + 2] : W[st][j], cs[j]);
      else cs[j] = mfma(W[st][j == 2 ? 4 : j], ones8, cs[j]);
    }
  };
  consumer_loop<10, K1_NS, K1_SLOT, 27, 14, KV ? 12 : 0>(lds, ld, mm, [&]() { TS(a.ws, 0, 1); });
  touch_done(tc[0]); touch_done(tc[1]);
  TS(a.ws, 0, 2);

  LAS float* mean = (LAS float*)(lds + K1_TAB);
  LAS float* rstd = mean + 128;
  LAS float* cst = mean + 256;
#pragma unroll
  for (int j = 0; j < 2; ++j) {
    const int r3 = t & 3;
    float a1 = s1[j][0], a2 = r3 == 0 ? s2[j][0] : r3 == 1 ? s2[j][1] : r3 == 2 ? s2[j][2] : s2[j][3];
    float m = a1 * (1.0f / 640.0f);
    float var = fmaxf(a2 * (1.0f / 640.0f) - m * m, 0.f);
    float r = __builtin_amdgcn_rsqf(var + 1e-5f);
    int row = wr * 64 + ((j + 2 * wc) & 3) * 16 + t;
    if (g == (t >> 2)) { mean[row] = m; rstd[row] = r; }
  }
#pragma unroll
  for (int j = 0; j < 3; ++j) {
    if constexpr (KV) {
      if (g == 0) cst[80 * wc + 16 * (j + 2 * wr) + t] = cs[j][0];
    } else {
      int col = (j == 2) ? 80 * wc + 64 + 4 * g : 80 * wc + (wr ? 32 : 0) + 8 * g + 4 * ((j ^ g) & 1);
      if (t == 0) *(LAS f32x4*)(cst + col) = cs[j];
    }
  }
  LGKM0();
  BAR();

  const rsrc_t rw = mk_rsrc(a.ws, WS_END);
  if constexpr (KV) {
    f32x4 M4[4], R4[4];
#pragma unroll
    for (int j = 0; j < 4; ++j) {
      int row = wr * 64 + ((j + 2 * wc) & 3) * 16 + 4 * g;
      M4[j] = *(LAS const f32x4*)(mean + row); R4[j] = *(LAS const f32x4*)(rstd + row);
    }
    bf16x8 op[2][5];
#pragma unroll
    for (int f = 0; f < 5; ++f) {
      const float c = cst[80 * wc + 16 * f + t];
      const bool isk = (f < 2) || (f == 2 && t < 8);
#pragma unroll
      for (int h = 0; h < 2; ++h) {
        u32x4 u;
#pragma unroll
        for (int jj = 0; jj < 2; ++jj) {
          const int j = 2 * h + jj;
          f32x2 y0 = lo2(R4[j]) * (lo2(acc[j][f]) - lo2(M4[j]) * c);
          f32x2 y1 = hi2(R4[j]) * (hi2(acc[j][f]) - hi2(M4[j]) * c);
          if (f <= 2) {
            f32x2 p0 = phi2(y0), p1 = phi2(y1);
            if (f < 2) { y0 = p0; y1 = p1; } else { y0 = isk ? p0 : y0; y1 = isk ? p1 : y1; }
          }
          u[2 * jj] = pack2v(y0); u[2 * jj + 1] = pack2v(y1);
        }
        op[h][f] = __builtin_bit_cast(bf16x8, u);
      }
    }
    const bf16x8 ones = __builtin_bit_cast(bf16x8, u32x4{ONES, ONES, ONES, ONES});
    f32x4 d[10];
    constexpr int pa[7] = {0, 0, 1, 1, 1, 2, 2}, pb[7] = {2, 3, 2, 3, 4, 3, 4};
#pragma unroll
    for (int k = 0; k < 7; ++k) { d[k] = mfma(op[0][pa[k]], op[0][pb[k]], f32x4{0.f, 0.f, 0.f, 0.f}); d[k] = mfma(op[1][pa[k]], op[1][pb[k]], d[k]); }
#pragma unroll
    for (int k = 0; k < 3; ++k) { d[7 + k] = mfma(op[0][k], ones, f32x4{0.f, 0.f, 0.f, 0.f}); d[7 + k] = mfma(op[1][k], ones, d[7 + k]); }
    LAS f32x4* red = (LAS f32x4*)lds;
#pragma unroll
    for (int k = 0; k < 10; ++k) red[(wid * 10 + k) * 64 + lane] = d[k];
    LGKM0();
    BAR();
    const unsigned kvp = WS_KVP + (panel * 32 + (ct - 4) * 4) * 420 * 2 + (wid * 64 + lane) * 4;
#pragma unroll
    for (int i = 0; i < 4; ++i) {
      float v0 = *(LAS const float*)(lds + el[i][0]) + *(LAS const float*)(lds + el[i][0] + 2 * 2560 * 4);
      float v1 = *(LAS const float*)(lds + el[i][1]) + *(LAS const float*)(lds + el[i][1] + 2 * 2560 * 4);
      if (i < 3 || wid * 64 + lane < 840 - 3 * 256) st4f<ST_AUX>(rw, kvp + i * 1024, __builtin_bit_cast(float, pack2(v0, v1)));
    }
  } else {
    constexpr bool isq = (MODE == 0);
    const int pr0 = wr ? 64 : 0, pr1 = wr ? 0 : 64;
    float m4[4], r4[4];
#pragma unroll
    for (int j = 0; j < 4; ++j) { int row = wr * 64 + ((j + 2 * wc) & 3) * 16 + t; m4[j] = mean[row]; r4[j] = rstd[row]; }
    if (ct == 12 && wc == 0 && g == 0) {
#pragma unroll
      for (int j = 0; j < 4; ++j) st8f<ST_AUX>(rw, WS_XST + (panel * 128 + wr * 64 + j * 16 + t) * 8, f32x2{m4[j], r4[j]});
    }
    f32x4 c4[5];
    {
      const int hE = 4 * (g & 1), hO = 4 - hE;
      c4[0] = *(LAS const f32x4*)(cst + 80 * wc + pr0 / 2 + 8 * g + hE);
      c4[1] = *(LAS const f32x4*)(cst + 80 * wc + pr0 / 2 + 8 * g + hO);
      c4[2] = *(LAS const f32x4*)(cst + 80 * wc + pr1 / 2 + 8 * g + hE);
      c4[3] = *(LAS const f32x4*)(cst + 80 * wc + pr1 / 2 + 8 * g + hO);
      c4[4] = *(LAS const f32x4*)(cst + 80 * wc + 64 + 4 * g);
    }
    unsigned obase, rstride;
    if (isq) { obase = WS_PQ + (2 * ct + wc) * (2048 * 160); rstride = 160; }
    else { obase = WS_HPAD + 2 * LD_A + (ct - 12) * 320 + wc * 160; rstride = LD_A; }
    const bool od = g & 1;
#pragma unroll
    for (int j = 0; j < 4; ++j) {
      const int tok = panel * 128 + wr * 64 + ((j + 2 * wc) & 3) * 16 + t;
      const float m = m4[j];
      float r = r4[j];
      if (!isq) { float var = fmaxf(1.0f - 1e-5f * r * r, 0.f); r *= __builtin_amdgcn_rsqf(var + 1e-5f); }
      unsigned pk[5][2];
#pragma unroll
      for (int f = 0; f < 5; ++f) {
        f32x2 y0 = (lo2(acc[j][f]) - lo2(c4[f]) * m) * r, y1 = (hi2(acc[j][f]) - hi2(c4[f]) * m) * r;
        pk[f][0] = pack2v(isq ? phi2(y0) : gelu2(y0)); pk[f][1] = pack2v(isq ? phi2(y1) : gelu2(y1));
      }
      const unsigned orow = obase + tok * rstride;
      st16<ST_AUX>(rw, orow + pr0 + 16 * g, od ? u32x4{pk[1][0], pk[1][1], pk[0][0], pk[0][1]} : u32x4{pk[0][0], pk[0][1], pk[1][0], pk[1][1]});
      st16<ST_AUX>(rw, orow + pr1 + 16 * g, od ? u32x4{pk[3][0], pk[3][1], pk[2][0], pk[2][1]} : u32x4{pk[2][0], pk[2][1], pk[3][0], pk[3][1]});
      st8<ST_AUX>(rw, orow + 128 + 8 * g, u32x2{pk[4][0], pk[4][1]});
    }
    if (!isq && (panel == 0 || panel == 15) && wid == 0 && lane < 40)
      st16<ST_AUX>(rw, WS_HPAD + (panel == 0 ? 0 : 2050) * LD_A + (ct - 12) * 320 + (lane / 20) * LD_A + (lane % 20) * 16, u32x4{0, 0, 0, 0});
  }
}

extern "C" __global__ __launch_bounds__(512) void k1_kernel(K1Args a) {
  extern __shared__ __attribute__((aligned(16))) char smem[];
  LAS char* lds = (LAS char*)smem;
  const int wid = __builtin_amdgcn_readfirstlane(threadIdx.x >> 6), lane = threadIdx.x & 63;
  const int xcd = blockIdx.x & 7, j = blockIdx.x >> 3;
  const int panel = (xcd & 1) * 8 + (j & 7), ct = (xcd >> 1) * 4 + (j >> 3);
  const bool kv = (ct >= 4 && ct < 12);
  TS(a.ws, 0, 0);
  if (wid >= 4) {
    const int w = wid - 4;
    unsigned voffA[4], voffB[5];
#pragma unroll
    for (int i = 0; i < 4; ++i) {
      int row = (w + 4 * i) * 8 + (lane >> 3), pc = (lane & 7) ^ ((row >> 1) & 7);
      voffA[i] = (panel * 128 + row) * 1280 + pc * 16;
    }
    const char* bB; unsigned ldb;
    if (ct < 4) { bB = a.wqkv + ct * 320; ldb = 3840; }
    else if (ct < 12) { bB = a.wqkv + 1280 + (ct - 4) * 160; ldb = 3840; }
    else { bB = a.w1 + (ct - 12) * 320; ldb = 1280; }
#pragma unroll
    for (int i = 0; i < 5; ++i) {
      int P = (w + 4 * i) * 64 + lane, k = P / 20, qq = P - k * 20;
      unsigned cb;
      if (kv) { int sp = qq ^ (((k >> 3) & 1) << 1); int blk = sp / 5, r5 = sp - blk * 5; cb = (blk & 1) * 1280 + (blk >> 1) * 80 + r5 * 16; }
      else { int sp = qq ^ ((k >> 3) & 1); cb = sp * 16; }
      voffB[i] = k * ldb + cb;
    }
    const int rot = ROT1 ? (((j & 7) * 5) / 4 + ((j >> 3) * 5) / 2) % 10 : 0;
    const char* bA = a.x;
    auto issue = [&](int k, int slot) {
      int c = k + rot; if (c >= 10) c -= 10;
      const char* pa = pin_s(bA + c * 128);
      const char* pb = pin_s(bB + (size_t)c * 64 * ldb);
      LAS char* sb = lds + slot * K1_SLOT + w * 1024;
#pragma unroll
      for (int i = 0; i < 4; ++i) glds16(pa + voffA[i], sb + i * 4096);
#pragma unroll
      for (int i = 0; i < 5; ++i) glds16(pb + voffB[i], sb + K1_AB + i * 4096);
    };
    loader_loop<K1_NS, 10, 9, 2, 1, (USE_PF ? 1 : 0) + (PFC ? 1 : 0)>(issue, [&]() {
      {
        const char* cb = CODE_ADDR(k2_kernel);
        long len = CODE_ADDR(kz_end) - cb;
        long off = (long)(w * 64 + lane) * 128;
        if (off >= len) off = 0;
        __builtin_amdgcn_global_load_lds((GAS const void*)(cb + off), (LAS void*)(lds + K1_DUMMY), 4, 0, 0);
      }
      int id = blockIdx.x * 256 + w * 64 + lane;
      if (id >= 43520) id -= 43520;
      const char* p;
      if (id < 6400) p = a.w2 + (size_t)id * 128;
      else if (id < 12800) p = a.wo + (size_t)(id - 6400) * 128;
      else if (id < 28160) p = a.w3 + (size_t)(id - 12800) * 128;
      else p = a.w4 + (size_t)(id - 28160) * 128;
      __builtin_amdgcn_global_load_lds((GAS const void*)p, (LAS void*)(lds + K1_DUMMY), 4, 0, 0);
    });
    return;
  }
  unsigned tc[2];
  tc[0] = prefetch_code(CODE_ADDR(k1_kernel), CODE_ADDR(k2_kernel), wid * 64 + lane);
  tc[1] = tc[0];
  {
    int id = blockIdx.x * 256 + wid * 64 + lane;
    if (id >= 46080) id -= 46080;
    const char* p;
    if (id < 20480) p = a.x + (size_t)id * 128;
    else if (id < 39680) p = a.wqkv + (size_t)(id - 20480) * 128;
    else p = a.w1 + (size_t)(id - 39680) * 128;
    tc[1] = touch(p);
  }
  if (kv) k1_consumer<1>(a, lds, wid, lane, panel, ct, tc);
  else if (ct < 4) k1_consumer<0>(a, lds, wid, lane, panel, ct, tc);
  else k1_consumer<2>(a, lds, wid, lane, panel, ct, tc);
  TS(a.ws, 0, 3);
}

constexpr int T_NS = 7, T_AB = 96 * 128, T_BB = 64 * 128, T_SLOT = T_AB + T_BB;
constexpr int T_DUMMY = T_NS * T_SLOT;
constexpr int T_LDS = T_DUMMY + 256;

template <int NC>
DEVI void t96_consumer(LAS char* lds, int wid, int lane, f32x4 (&acc)[3][2], char* ws, int kk, unsigned tc) {
  const int wr = wid >> 1, wc = wid & 1, g = lane >> 4, t = lane & 15, sw = t >> 1;
  unsigned aoff[2];
#pragma unroll
  for (int s = 0; s < 2; ++s) aoff[s] = (wr * 48 + t) * 128 + (((4 * s + g) ^ sw) * 16);
  const int q = t >> 2, p = lane & 3;
  const int swz = (((q >> 1) & 1) << 2) | (g & 1);
  const unsigned rowb = T_AB + (8 * g + q) * 128 + (((4 * wc + p) ^ swz) * 16);
  const unsigned bE = rowb + ((p & 1) ? 8 : 0), bO = rowb + ((p & 1) ? 0 : 8);
#pragma unroll
  for (int i = 0; i < 3; ++i) { acc[i][0] = f32x4{0.f, 0.f, 0.f, 0.f}; acc[i][1] = acc[i][0]; pin_v(acc[i][0]); pin_v(acc[i][1]); }
  bf16x8 X[2][2][3], W[2][2][2];
  auto ld = [&](auto SET, LAS const char* sb) {
    constexpr int st = decltype(SET)::value;
#pragma unroll
    for (int s = 0; s < 2; ++s) {
      X[st][s][0] = ld_frag(sb + aoff[s]);
      W[st][s][0] = ld_tr(sb + bE + s * 4096, sb + bE + s * 4096 + 512);
      W[st][s][1] = ld_tr(sb + bO + s * 4096, sb + bO + s * 4096 + 512);
      X[st][s][1] = ld_frag(sb + aoff[s] + 2048);
      X[st][s][2] = ld_frag(sb + aoff[s] + 4096);
    }
  };
  auto mm = [&](auto SET) {
    constexpr int st = decltype(SET)::value;
#pragma unroll
    for (int s = 0; s < 2; ++s)
#pragma unroll
      for (int i = 0; i < 3; ++i) { acc[i][0] = mfma(W[st][s][0], X[st][s][i], acc[i][0]); acc[i][1] = mfma(W[st][s][1], X[st][s][i], acc[i][1]); }
  };
  consumer_loop_full<NC, T_NS, T_SLOT, 12, 14>(lds, ld, mm, [&]() { TS(ws, kk, 1); });
  touch_done(tc);
  TS(ws, kk, 2);
}

template <int NC, bool SHIFT>
DEVI void t96_loader(LAS char* lds, int w, int lane, const char* A, unsigned lda, int row0, const char* B, unsigned ldb, int rot) {
  unsigned voffA[3], voffB[2];
  const int pslot = lane & 7;
  int pcs[3];
#pragma unroll
  for (int i = 0; i < 3; ++i) {
    int row = (w + 4 * i) * 8 + (lane >> 3), pc = pslot ^ ((row >> 1) & 7);
    int gr = row0 + row; if (gr > S_ - 1) gr = S_ - 1;
    voffA[i] = gr * lda + pc * 16; pcs[i] = pc * 8;
  }
#pragma unroll
  for (int i = 0; i < 2; ++i) {
    int k = (w + 4 * i) * 8 + (lane >> 3);
    int sp = pslot ^ ((((k >> 1) & 1) << 2) | ((k >> 3) & 1));
    voffB[i] = k * ldb + sp * 16;
  }
  auto issue = [&](int k, int slot) {
    int c = k + rot; if (c >= NC) c -= NC;
    const char* pa = pin_s(A + c * 128);
    const char* pb = pin_s(B + (size_t)c * 64 * ldb);
    LAS char* sb = lds + slot * T_SLOT + w * 1024;
#pragma unroll
    for (int i = 0; i < 3; ++i) {
      unsigned vo = voffA[i];
      if constexpr (SHIFT) {
        int kc = c * 64 + pcs[i];
        vo += (kc < 160) ? LD_A : (kc < 320) ? 3 * LD_A : (kc < 480) ? 0 : 4 * LD_A;
      }
      glds16(pa + vo, sb + i * 4096);
    }
#pragma unroll
    for (int i = 0; i < 2; ++i) glds16(pb + voffB[i], sb + T_AB + i * 4096);
  };
  loader_loop<T_NS, NC, 5, T_R, 2, 0>(issue, []() {});
}

DEVI void pair8(const f32x4& e, const f32x4& o, int g, f32x2 (&v)[4]) {
  const bool od = g & 1;
  v[0] = od ? lo2(o) : lo2(e); v[1] = od ? hi2(o) : hi2(e); v[2] = od ? lo2(e) : lo2(o); v[3] = od ? hi2(e) : hi2(o);
}
DEVI void t96_map(int tile, int& panel, int& ct, int& pin) {
  int grp = tile / 40, rem = tile - grp * 40;
  if (grp < 5) { ct = rem >> 2; pin = rem & 3; } else { ct = rem >> 1; pin = rem & 1; }
  panel = grp * 4 + pin;
}

struct K2Args { const char* x; const char* w2; char* ws; };

DEVI void k2_side(const K2Args& a, LAS char* lds, int wid, int lane, int hg, int tq) {
  __builtin_amdgcn_s_waitcnt(0x0F70);
  const int tid = wid * 64 + lane, g = lane >> 4, t = lane & 15;
  u32x2 v[16];
  const int kt = tid < 420 ? tid : 0;
  {
    const rsrc_t rk = mk_rsrc(a.ws + WS_KVP, 16 * 32 * 420 * 2);
#pragma unroll
    for (int i = 0; i < 16; ++i) v[i] = __builtin_amdgcn_raw_buffer_load_b64(rk, (hg * 420 + kt) * 8, i * 32 * 840, 0);
  }
  *(LAS u32x4*)(lds + tid * 16) = u32x4{0, 0, 0, 0};
  if (tid < 16) *(LAS u32x4*)(lds + 8192 + tid * 16) = u32x4{0, 0, 0, 0};
  LGKM0();
  BAR();
  const rsrc_t rq = mk_rsrc(a.ws + WS_PQ + (size_t)hg * 2048 * 160, 2048 * 160);
  u32x4 B[4][4];
#pragma unroll
  for (int i = 0; i < 4; ++i)
#pragma unroll
    for (int j = 0; j < 4; ++j) B[i][j] = ld16(rq, (tq * 512 + wid * 64 + i * 16 + t) * 160 + 40 * j + 16 * g);
  SB0();
  {
    f32x2 sa[4], sb[4];
#pragma unroll
    for (int i = 0; i < 4; ++i) {
      sa[i] = (unpack2(v[i][0]) + unpack2(v[i + 4][0])) + (unpack2(v[i + 8][0]) + unpack2(v[i + 12][0]));
      sb[i] = (unpack2(v[i][1]) + unpack2(v[i + 4][1])) + (unpack2(v[i + 8][1]) + unpack2(v[i + 12][1]));
    }
    const f32x2 s0 = (sa[0] + sa[1]) + (sa[2] + sa[3]), s1 = (sb[0] + sb[1]) + (sb[2] + sb[3]);
    const int hd = kt / 105, r4 = (kt - hd * 105) * 4;
    unsigned p0 = pack2v(s0), p1 = pack2v(s1);
    pin_v(p0); pin_v(p1);
    if (tid < 420) {
      if (r4 < 400) {
        const int d = r4 / 20, e = r4 - d * 20;
        const int row = e < 16 ? e : 4 * hd + e - 16;
        LAS char* q = lds + hd * 2048 + (e < 16 ? 0 : 1024) + ((d >> 3) * 16 + row) * 16 + (d & 7) * 2;
        *(LAS unsigned short*)(q) = (unsigned short)p0; *(LAS unsigned short*)(q + 16) = (unsigned short)(p0 >> 16);
        *(LAS unsigned short*)(q + 32) = (unsigned short)p1; *(LAS unsigned short*)(q + 48) = (unsigned short)(p1 >> 16);
      } else {
        *(LAS u32x2*)(lds + 8192 + hd * 64 + (r4 - 400) * 2) = u32x2{p0, p1};
      }
    }
  }
  LGKM0();
  BAR();
  bf16x8 A[4][3];
#pragma unroll
  for (int j = 0; j < 4; ++j) {
    A[j][0] = ld_frag(lds + j * 2048 + lane * 16);
    A[j][1] = ld_frag(lds + j * 2048 + 1024 + lane * 16);
    A[j][2] = ld_frag(lds + 8192 + j * 64 + g * 16);
  }
  const unsigned m01 = g < 3 ? ~0u : 0u, m23 = g < 2 ? ~0u : 0u;
  const rsrc_t rw = mk_rsrc(a.ws, WS_END);
#pragma unroll
  for (int i = 0; i < 4; ++i) {
    const f32x4 z4 = {0.f, 0.f, 0.f, 0.f};
    f32x4 d0[4], dz[4], d1 = z4;
#pragma unroll
    for (int j = 0; j < 4; ++j) {
      const bf16x8 b = __builtin_bit_cast(bf16x8, u32x4{B[i][j][0] & m01, B[i][j][1] & m01, B[i][j][2] & m23, B[i][j][3] & m23});
      dz[j] = mfma(A[j][2], b, z4); d0[j] = mfma(A[j][0], b, z4); d1 = mfma(A[j][1], b, d1);
    }
    float inv[4]; unsigned pk[4][2];
#pragma unroll
    for (int j = 0; j < 4; ++j) {
      inv[j] = __builtin_amdgcn_rcpf(dz[j][0] + 1e-6f);
      pk[j][0] = pack2v(lo2(d0[j]) * inv[j]); pk[j][1] = pack2v(hi2(d0[j]) * inv[j]);
    }
    const float ig = g == 0 ? inv[0] : g == 1 ? inv[1] : g == 2 ? inv[2] : inv[3];
    const unsigned orow = WS_O + (tq * 512 + wid * 64 + i * 16 + t) * LD_A;
    st16<ST_AUX>(rw, orow + hg * 128 + 16 * g, u32x4{pk[0][0], pk[0][1], pk[1][0], pk[1][1]});
    st16<ST_AUX>(rw, orow + hg * 128 + 64 + 16 * g, u32x4{pk[2][0], pk[2][1], pk[3][0], pk[3][1]});
    st8<ST_AUX>(rw, orow + 1024 + hg * 32 + 8 * g, u32x2{pack2v(lo2(d1) * ig), pack2v(hi2(d1) * ig)});
  }
}
DEVI int o_row_lo(int k) { return 20 * (2 * (k >> 5) + ((k >> 2) & 1)) + 4 * ((k >> 3) & 3) + (k & 3); }
DEVI int o_row_hi(int k) { return 80 * (k >> 4) + 20 * ((k >> 2) & 3) + 16 + (k & 3); }

extern "C" __global__ __launch_bounds__(512) void k2_kernel(K2Args a) {
  extern __shared__ __attribute__((aligned(16))) char smem[];
  LAS char* lds = (LAS char*)smem;
  const int wid = __builtin_amdgcn_readfirstlane(threadIdx.x >> 6), lane = threadIdx.x & 63;
  const int xcd = blockIdx.x & 7, j = blockIdx.x >> 3;
  TS(a.ws, 1, 0);
  if (j >= 28) { int sidx = (j - 28) * 8 + xcd; k2_side(a, lds, wid, lane, sidx & 7, sidx >> 3); TS(a.ws, 1, 3); return; }
  const int tile = xcd * 28 + j;
  if (tile >= 220) return;
  int panel, ct, pin;
  t96_map(tile, panel, ct, pin);
  if (wid >= 4) {
    t96_loader<10, true>(lds, wid - 4, lane, a.ws + WS_HPAD, LD_A, panel * 96, a.w2 + ct * 128, 1280, USE_ROT ? (ct + (pin * 5) / 2) % 10 : 0);
    return;
  }
  const unsigned tc = prefetch_code(CODE_ADDR(k2_kernel), CODE_ADDR(k3_kernel), wid * 64 + lane);
  const int wr = wid >> 1, wc = wid & 1, g = lane >> 4, t = lane & 15;
  const rsrc_t rx = mk_rsrc(a.x, S_ * 1280), rw = mk_rsrc(a.ws, WS_END);
  u32x4 xv[3]; f32x2 st[3]; int tok[3];
#pragma unroll
  for (int i = 0; i < 3; ++i) {
    tok[i] = panel * 96 + wr * 48 + i * 16 + t;
    int tk = tok[i] < S_ ? tok[i] : S_ - 1;
    xv[i] = ld16(rx, tk * 1280 + ct * 128 + wc * 64 + g * 16);
    st[i] = ld8f(rw, WS_XST + tk * 8);
  }
  f32x4 acc[3][2];
  t96_consumer<10>(lds, wid, lane, acc, a.ws, 1, tc);
#pragma unroll
  for (int i = 0; i < 3; ++i) {
    f32x2 v[4];
    pair8(acc[i][0], acc[i][1], g, v);
    u32x4 o; float s1 = 0.f, s2 = 0.f;
#pragma unroll
    for (int e = 0; e < 4; ++e) {
      unsigned xn = pack2v((unpack2(xv[i][e]) - st[i][0]) * st[i][1]);
      o[e] = pack2v(unpack2(xn) + v[e]);
      s1 = dot2acc(o[e], 0x3f803f80u, s1); s2 = dot2acc(o[e], o[e], s2);
    }
    s1 = red_g(s1); s2 = red_g(s2);
    if (tok[i] < S_) {
      st16<ST_AUX>(rw, WS_X1 + tok[i] * LD_A + ct * 128 + wc * 64 + g * 16, o);
      if (g == 0) st8f<ST_AUX>(rw, WS_X1P + ((2 * ct + wc) * 2048 + tok[i]) * 8, f32x2{s1, s2});
    }
  }
  TS(a.ws, 1, 3);
}

constexpr int K3_NS = 4, K3_AB = 144 * 128, K3_BB = 64 * 256, K3_SLOT = K3_AB + K3_BB;
constexpr int K3_TAB = K3_NS * K3_SLOT;
constexpr int K3_DUMMY = K3_TAB + 144 * 8;
constexpr int K3_LDS = K3_DUMMY + 256;

struct K3Args { const char* w3; const char* wo; char* ws; };

template <bool MLP>
DEVI void k3_consumer(const K3Args& a, LAS char* lds, int wid, int lane, int panel, int ct, unsigned tc) {
  const int g = lane >> 4, t = lane & 15, sw = t >> 1;
  LAS f32x2* stat = (LAS f32x2*)(lds + K3_TAB);
  const rsrc_t rw = mk_rsrc(a.ws, WS_END);
  f32x2 part[20];
  const int tid = wid * 64 + lane;
  if constexpr (MLP) {
    int tk = panel * 144 + (tid < 144 ? tid : 0); if (tk > S_ - 1) tk = S_ - 1;
    if (wid < 3) {
#pragma unroll
      for (int i = 0; i < 20; ++i) part[i] = ld8f(rw, WS_X1P + (i * 2048 + tk) * 8);
    }
  }
  unsigned aoff[2];
#pragma unroll
  for (int s = 0; s < 2; ++s) aoff[s] = t * 128 + (((4 * s + g) ^ sw) * 16);
  const int q = t >> 2, p = lane & 3;
  const int swz = (q << 2) | (g & 1);
  const unsigned rowb = K3_AB + (8 * g + q) * 256 + (((4 * wid + p) ^ swz) * 16);
  const unsigned bE = rowb + ((p & 1) ? 8 : 0), bO = rowb + ((p & 1) ? 0 : 8);
  f32x4 acc[9][2];
#pragma unroll
  for (int i = 0; i < 9; ++i) { acc[i][0] = f32x4{0.f, 0.f, 0.f, 0.f}; acc[i][1] = acc[i][0]; pin_v(acc[i][0]); pin_v(acc[i][1]); }
  f32x4 cE = {0.f, 0.f, 0.f, 0.f}, cO = cE;
  bf16x8 ones8 = __builtin_bit_cast(bf16x8, u32x4{0x3f803f80u, 0x3f803f80u, 0x3f803f80u, 0x3f803f80u});
  if constexpr (MLP) { pin_v(cE); pin_v(cO); pin_v(ones8); }
  bf16x8 X[2][9], W[2][2];
  auto ld = [&](auto SET, LAS const char* sb, auto SS) {
    constexpr int st = decltype(SET)::value, s = decltype(SS)::value;
    X[st][0] = ld_frag(sb + aoff[s]);
    W[st][0] = ld_tr(sb + bE + s * 8192, sb + bE + s * 8192 + 1024);
    W[st][1] = ld_tr(sb + bO + s * 8192, sb + bO + s * 8192 + 1024);
#pragma unroll
    for (int j = 1; j < 9; ++j) X[st][j] = ld_frag(sb + aoff[s] + j * 2048);
  };
  auto mm = [&](auto SET) {
    constexpr int st = decltype(SET)::value;
#pragma unroll
    for (int i = 0; i < 9; ++i) { acc[i][0] = mfma(W[st][0], X[st][i], acc[i][0]); acc[i][1] = mfma(W[st][1], X[st][i], acc[i][1]); }
    if constexpr (MLP) { cE = mfma(W[st][0], ones8, cE); cO = mfma(W[st][1], ones8, cO); }
  };
  auto pre = [&]() {
    TS(a.ws, 2, 1);
    if constexpr (MLP) {
      if (wid < 3) {
#pragma unroll
        for (int i = 0; i < 10; ++i) part[i] += part[i + 10];
#pragma unroll
        for (int i = 0; i < 5; ++i) part[i] += part[i + 5];
        f32x2 s = (part[0] + part[1]) + (part[2] + part[3]) + part[4];
        float m = s[0] * (1.0f / 640.0f);
        float var = fmaxf(s[1] * (1.0f / 640.0f) - m * m, 0.f);
        if (tid < 144) stat[tid] = f32x2{m, __builtin_amdgcn_rsqf(var + 1e-5f)};
      }
    }
  };
  consumer_loop<10, K3_NS, K3_SLOT, MLP ? 20 : 18, 13, 0>(lds, ld, mm, pre);
  touch_done(tc);
  TS(a.ws, 2, 2);
  const unsigned obase = MLP ? WS_HID + ct * 256 + wid * 64 + g * 16 : WS_S + (ct - 12) * 256 + wid * 64 + g * 16;
  const unsigned rs = MLP ? LD_HID : 1280;
  const bool od = g & 1;
#pragma unroll
  for (int i = 0; i < 9; ++i) {
    const int tok = panel * 144 + i * 16 + t;
    f32x2 e0 = lo2(acc[i][0]), e1 = hi2(acc[i][0]), o0 = lo2(acc[i][1]), o1 = hi2(acc[i][1]);
    if constexpr (MLP) {
      const f32x2 st = stat[i * 16 + t];
      e0 = gelu2((e0 - lo2(cE) * st[0]) * st[1]); e1 = gelu2((e1 - hi2(cE) * st[0]) * st[1]);
      o0 = gelu2((o0 - lo2(cO) * st[0]) * st[1]); o1 = gelu2((o1 - hi2(cO) * st[0]) * st[1]);
    }
    unsigned pe0 = pack2v(e0), pe1 = pack2v(e1), po0 = pack2v(o0), po1 = pack2v(o1);
    if (tok < S_) st16<ST_AUX>(rw, obase + tok * rs, od ? u32x4{po0, po1, pe0, pe1} : u32x4{pe0, pe1, po0, po1});
  }
}

extern "C" __global__ __launch_bounds__(512) void k3_kernel(K3Args a) {
  extern __shared__ __attribute__((aligned(16))) char smem[];
  LAS char* lds = (LAS char*)smem;
  const int wid = __builtin_amdgcn_readfirstlane(threadIdx.x >> 6), lane = threadIdx.x & 63;
  const int xcd = blockIdx.x & 7, j = blockIdx.x >> 3;
  const int tile = xcd * 32 + j;
  if (tile >= 255) return;
  const int grp = tile / 68, rem = tile - grp * 68;
  int ct, pin;
  if (grp < 3) { ct = rem >> 2; pin = rem & 3; } else { ct = rem / 3; pin = rem - ct * 3; }
  const int panel = grp * 4 + pin;
  const bool mlp = ct < 12;
  TS(a.ws, 2, 0);
  if (wid >= 4) {
    const int w = wid - 4;
    const char* A = mlp ? a.ws + WS_X1 : a.ws + WS_O;
    const char* B = mlp ? a.w3 + ct * 256 : a.wo + (ct - 12) * 256;
    const unsigned ldb = mlp ? 3072 : 1280;
    unsigned voffA[5], voffB[4], voffB2[4];
#pragma unroll
    for (int i = 0; i < 5; ++i) {
      int ii = w + 4 * i; if (ii > 17) ii = 17;
      int row = ii * 8 + (lane >> 3), pc = (lane & 7) ^ ((row >> 1) & 7);
      int gr = panel * 144 + row; if (gr > S_ - 1) gr = S_ - 1;
      voffA[i] = gr * LD_A + pc * 16;
    }
#pragma unroll
    for (int i = 0; i < 4; ++i) {
      int P = (w + 4 * i) * 64 + lane, k = P >> 4, qq = P & 15;
      int sp = qq ^ (((k & 3) << 2) | ((k >> 3) & 1));
      voffB[i] = (mlp ? k : o_row_lo(k)) * ldb + sp * 16;
      voffB2[i] = (mlp ? k : o_row_hi(k)) * ldb + sp * 16;
    }
    const int last = (w + 16 > 17) ? 17 : w + 16;
    const int rot = USE_ROT ? ((pin * 5) / 2 + ((ct & 7) * 5) / 4) % 10 : 0;
    auto issue = [&](int k, int slot) {
      int c = k + rot; if (c >= 10) c -= 10;
      const char* pa = pin_s(A + c * 128);
      const char* pb = pin_s(B + (size_t)(mlp ? c * 64 : c < 8 ? c * 80 : (c - 8) * 320) * ldb);
      LAS char* sb = lds + slot * K3_SLOT;
#pragma unroll
      for (int i = 0; i < 4; ++i) glds16(pa + voffA[i], sb + (w + 4 * i) * 1024);
      glds16(pa + voffA[4], sb + last * 1024);
#pragma unroll
      for (int i = 0; i < 4; ++i) glds16(pb + (c < 8 ? voffB[i] : voffB2[i]), sb + K3_AB + (w + 4 * i) * 1024);
    };
    loader_loop<K3_NS, 10, 9, 2, 1, 0>(issue, []() {});
    return;
  }
  const unsigned tc = prefetch_code(CODE_ADDR(k3_kernel), CODE_ADDR(k4_kernel), wid * 64 + lane);
  if (mlp) k3_consumer<true>(a, lds, wid, lane, panel, ct, tc);
  else k3_consumer<false>(a, lds, wid, lane, panel, ct, tc);
  TS(a.ws, 2, 3);
}

struct K4Args { const char* x; const char* w4; char* ws; char* out; };

extern "C" __global__ __launch_bounds__(512) void k4_kernel(K4Args a) {
  extern __shared__ __attribute__((aligned(16))) char smem[];
  LAS char* lds = (LAS char*)smem;
  const int wid = __builtin_amdgcn_readfirstlane(threadIdx.x >> 6), lane = threadIdx.x & 63;
  const int xcd = blockIdx.x & 7, j = blockIdx.x >> 3;
  const int tile = xcd * 28 + j;
  if (j >= 28 || tile >= 220) return;
  int panel, ct, pin;
  t96_map(tile, panel, ct, pin);
  TS(a.ws, 3, 0);
  if (wid >= 4) {
    t96_loader<24, false>(lds, wid - 4, lane, a.ws + WS_HID, LD_HID, panel * 96, a.w4 + ct * 128, 1280, USE_ROT ? (pin * 6 + (ct * 24) / 7) % 24 : 0);
    return;
  }
  const unsigned tc = prefetch_code(CODE_ADDR(k4_kernel), CODE_ADDR(kz_end), wid * 64 + lane);
  const int wr = wid >> 1, wc = wid & 1, g = lane >> 4, t = lane & 15;
  const rsrc_t rx = mk_rsrc(a.x, S_ * 1280), rw = mk_rsrc(a.ws, WS_END), ro = mk_rsrc(a.out, S_ * 1280);
  u32x4 xv[3], x1v[3], sv[3]; int tok[3];
#pragma unroll
  for (int i = 0; i < 3; ++i) {
    tok[i] = panel * 96 + wr * 48 + i * 16 + t;
    int tk = tok[i] < S_ ? tok[i] : S_ - 1;
    unsigned off = tk * 1280 + ct * 128 + wc * 64 + g * 16;
    xv[i] = ld16(rx, off);
    x1v[i] = ld16(rw, WS_X1 + tk * LD_A + ct * 128 + wc * 64 + g * 16);
    sv[i] = ld16(rw, WS_S + off);
  }
  f32x4 acc[3][2];
  t96_consumer<24>(lds, wid, lane, acc, a.ws, 3, tc);
#pragma unroll
  for (int i = 0; i < 3; ++i) {
    f32x2 v[4];
    pair8(acc[i][0], acc[i][1], g, v);
    u32x4 o;
#pragma unroll
    for (int e = 0; e < 4; ++e) o[e] = pack2v(unpack2(xv[i][e]) + unpack2(sv[i][e]) * silu2(unpack2(x1v[i][e]) + v[e]));
    st16<OUT_AUX>(ro, tok[i] * 1280 + ct * 128 + wc * 64 + g * 16, o);
  }
  TS(a.ws, 3, 3);
}

extern "C" __global__ void kz_end() {}

extern "C" void kernel_launch(void* const* d_in, const int* in_sizes, int n_in, void* d_out, int out_size, void* d_ws, size_t ws_size,
                              hipStream_t stream) {
  const char* x = (const char*)d_in[0];
  const char* wqkv = (const char*)d_in[1];
  const char* wo = (const char*)d_in[2];
  const char* w1 = (const char*)d_in[4];
  const char* w2 = (const char*)d_in[5];
  const char* w3 = (const char*)d_in[6];
  const char* w4 = (const char*)d_in[7];
  char* ws = (char*)d_ws;
  (void)hipFuncSetAttribute((const void*)k1_kernel, hipFuncAttributeMaxDynamicSharedMemorySize, K1_LDS);
  (void)hipFuncSetAttribute((const void*)k2_kernel, hipFuncAttributeMaxDynamicSharedMemorySize, T_LDS);
  (void)hipFuncSetAttribute((const void*)k3_kernel, hipFuncAttributeMaxDynamicSharedMemorySize, K3_LDS);
  (void)hipFuncSetAttribute((const void*)k4_kernel, hipFuncAttributeMaxDynamicSharedMemorySize, T_LDS);
  k1_kernel<<<256, 512, K1_LDS, stream>>>(K1Args{x, wqkv, w1, w2, w3, w4, wo, ws});
  k2_kernel<<<256, 512, T_LDS, stream>>>(K2Args{x, w2, ws});
  k3_kernel<<<256, 512, K3_LDS, stream>>>(K3Args{w3, wo, ws});
  k4_kernel<<<256, 512, T_LDS, stream>>>(K4Args{x, w4, ws, (char*)d_out});
}
```

```cpp
#include <hip/hip_runtime.h>
#include <stdint.h>

#define LAS __attribute__((address_space(3)))
#define GAS __attribute__((address_space(1)))
#define DEVI __device__ __forceinline__

typedef __bf16 bf16x2 __attribute__((ext_vector_type(2)));
typedef __bf16 bf16x8 __attribute__((ext_vector_type(8)));
typedef float f32x2 __attribute__((ext_vector_type(2)));
typedef float f32x4 __attribute__((ext_vector_type(4)));
typedef short s16x4 __attribute__((ext_vector_type(4)));
typedef unsigned u32x2 __attribute__((ext_vector_type(2)));
typedef unsigned u32x4 __attribute__((ext_vector_type(4)));
typedef __amdgpu_buffer_rsrc_t rsrc_t;

#define ST_AUX 16
#define OUT_T 0
#define OUT_AUX 16
#define USE_ROT 0
#define USE_PF 1

#define T_R 3
#define ROT1 0
#define CM2 0
#define CM4 0
#define NB2 0
#define NB4 0
#define ROT4 0
#define TLBW 0
#define PF1 1
#define PF1S 0
#define PF1D 0
#define PF1U 2
#define PF1C 4
#define SIDE1ST 0
#define SMASK 0
#define GLAY 1
#define HLAY 1
#define OLAY 1
constexpr int S_ = 2048;

#define LD_A 1408
#define LD_HID 3200
constexpr unsigned WS_HPAD = 0;
constexpr unsigned WS_PQ   = WS_HPAD + 2052 * LD_A;
constexpr unsigned WS_KVP  = WS_PQ + 2048 * 640 * 2;
constexpr unsigned WS_XST  = WS_KVP + 16 * 32 * 420 * 4;
constexpr unsigned WS_X1   = WS_XST + 2048 * 8;
constexpr unsigned WS_X1P  = WS_X1 + 2048 * LD_A;
constexpr unsigned WS_O    = WS_X1P + 20 * 2048 * 8;
constexpr unsigned WS_HID  = WS_O + 2048 * LD_A;
constexpr unsigned WS_S    = WS_HID + 2048 * LD_HID;
constexpr unsigned WS_OHI  = WS_S + 2048 * 640 * 2;
constexpr unsigned WS_END  = WS_OHI + 8 * 2048 * 32;
constexpr unsigned H_GS = 2052 * 64, H_GS2 = 2056 * 32;
constexpr unsigned WS_HHI = WS_HPAD + 16 * H_GS, PQ_HI = 16 * 131072;

template <int V> struct IC { static constexpr int value = V; };
DEVI unsigned pack2(float a, float b) { f32x2 v = {a, b}; return __builtin_bit_cast(unsigned, __builtin_convertvector(v, bf16x2)); }
DEVI unsigned pack2v(f32x2 v) { return __builtin_bit_cast(unsigned, __builtin_convertvector(v, bf16x2)); }
DEVI float bflo(unsigned u) { return __builtin_bit_cast(float, u << 16); }
DEVI float bfhi(unsigned u) { return __builtin_bit_cast(float, u & 0xffff0000u); }
DEVI f32x2 unpack2(unsigned u) { return f32x2{bflo(u), bfhi(u)}; }
DEVI float dot2acc(unsigned a, unsigned b, float c) { return __builtin_amdgcn_fdot2_f32_bf16(__builtin_bit_cast(bf16x2, a), __builtin_bit_cast(bf16x2, b), c, false); }
DEVI f32x2 lo2(f32x4 v) { return f32x2{v[0], v[1]}; }
DEVI f32x2 hi2(f32x4 v) { return f32x2{v[2], v[3]}; }
DEVI f32x2 gelu2(f32x2 u) {
  const float c1 = -2.3022081986f, c2 = -0.10294324f;
  f32x2 w = u * (u * u * c2 + c1);
  f32x2 d = f32x2{__builtin_amdgcn_exp2f(w[0]), __builtin_amdgcn_exp2f(w[1])} + 1.0f;
  return u * f32x2{__builtin_amdgcn_rcpf(d[0]), __builtin_amdgcn_rcpf(d[1])};
}
DEVI f32x2 silu2(f32x2 b) {
  f32x2 w = b * -1.4426950409f;
  f32x2 d = f32x2{__builtin_amdgcn_exp2f(w[0]), __builtin_amdgcn_exp2f(w[1])} + 1.0f;
  return b * f32x2{__builtin_amdgcn_rcpf(d[0]), __builtin_amdgcn_rcpf(d[1])};
}
DEVI f32x2 phi2(f32x2 t) {
  f32x2 w = t * 1.4426950409f;
  f32x2 e = {__builtin_amdgcn_exp2f(fminf(w[0], 0.f)), __builtin_amdgcn_exp2f(fminf(w[1], 0.f))};
  return (f32x2{fmaxf(t[0], 0.f), fmaxf(t[1], 0.f)} + e) + 1e-6f;
}
DEVI float red_g(float v) { v += __shfl_xor(v, 16); v += __shfl_xor(v, 32); return v; }
DEVI f32x4 mfma(bf16x8 a, bf16x8 b, f32x4 c) { return __builtin_amdgcn_mfma_f32_16x16x32_bf16(a, b, c, 0, 0, 0); }
DEVI bf16x8 ld_frag(LAS const char* p) { return *(LAS const bf16x8*)p; }
DEVI bf16x8 ld_tr(LAS const char* p0, LAS const char* p1) {
  s16x4 a = __builtin_amdgcn_ds_read_tr16_b64_v4i16((LAS s16x4*)p0);
  s16x4 b = __builtin_amdgcn_ds_read_tr16_b64_v4i16((LAS s16x4*)p1);
  return __builtin_bit_cast(bf16x8, __builtin_shufflevector(a, b, 0, 1, 2, 3, 4, 5, 6, 7));
}
DEVI void glds16(const char* g, LAS char* l) { __builtin_amdgcn_global_load_lds((GAS const void*)g, (LAS void*)l, 16, 0, 0); }
DEVI rsrc_t mk_rsrc(const void* p, unsigned bytes) { return __builtin_amdgcn_make_buffer_rsrc((void*)p, 0, (int)bytes, 0x00020000); }
template <int AUX> DEVI void st16(rsrc_t r, unsigned off, u32x4 v) { __builtin_amdgcn_raw_buffer_store_b128(v, r, off, 0, AUX); }
template <int AUX> DEVI void st8(rsrc_t r, unsigned off, u32x2 v) { __builtin_amdgcn_raw_buffer_store_b64(v, r, off, 0, AUX); }
template <int AUX> DEVI void st8f(rsrc_t r, unsigned off, f32x2 v) { __builtin_amdgcn_raw_buffer_store_b64(__builtin_bit_cast(u32x2, v), r, off, 0, AUX); }
template <int AUX> DEVI void st4f(rsrc_t r, unsigned off, float v) { __builtin_amdgcn_raw_buffer_store_b32(__builtin_bit_cast(unsigned, v), r, off, 0, AUX); }
DEVI u32x4 ld16(rsrc_t r, unsigned off) { return __builtin_amdgcn_raw_buffer_load_b128(r, off, 0, 0); }
DEVI f32x2 ld8f(rsrc_t r, unsigned off) { return __builtin_bit_cast(f32x2, __builtin_amdgcn_raw_buffer_load_b64(r, off, 0, 0)); }
#define CBAR() asm volatile("" ::: "memory")
#define BAR() do { CBAR(); __builtin_amdgcn_s_barrier(); CBAR(); } while (0)
#define SB0() __builtin_amdgcn_sched_barrier(0)
#define LGKM0() __builtin_amdgcn_s_waitcnt(0xC07F)
#define WAITV(n) asm volatile("s_waitcnt vmcnt(%0)" ::"i"(n) : "memory")
DEVI const char* pin_s(const char* p) { asm volatile("" : "+s"(p)); return p; }
template <class T> DEVI void pin_v(T& v) { asm volatile("" : "+v"(v)); }

#define PFC 1
#define CODE_ADDR(sym) ({ unsigned lo_, hi_; asm volatile("s_getpc_b64 s[40:41]\n\ts_add_u32 s40, s40, " #sym "@rel32@lo+4\n\ts_addc_u32 s41, s41, " #sym "@rel32@hi+12\n\ts_mov_b32 %0, s40\n\ts_mov_b32 %1, s41" : "=s"(lo_), "=s"(hi_) : : "s40", "s41", "scc"); (const char*)(((unsigned long long)hi_ << 32) | lo_); })
DEVI unsigned touch(const char* p) { unsigned d; asm volatile("global_load_dword %0, %1, off" : "=v"(d) : "v"(p)); return d; }
#define PFM ""
DEVI unsigned touch_m(const char* p) { unsigned d; asm volatile("global_load_dword %0, %1, off" PFM : "=v"(d) : "v"(p)); return d; }
DEVI void touch_s(unsigned& d, unsigned voff, const char* base) { asm volatile("global_load_dword %0, %1, %2" : "+v"(d) : "v"(voff), "s"(base)); }
DEVI void touch_done(unsigned d) { __builtin_amdgcn_s_waitcnt(0x0F70); asm volatile("" : : "v"(d)); }
template <int N> DEVI void touch_done(const unsigned (&d)[N]) {
  __builtin_amdgcn_s_waitcnt(0x0F70);
#pragma unroll
  for (int i = 0; i < N; ++i) asm volatile("" : : "v"(d[i]));
}
DEVI unsigned prefetch_code(const char* beg, const char* end, int idx) {
  long len = end - beg, off = (long)idx * 128;
  return touch(beg + (off < len ? off : 0));
}

#define TS(ws, k, j) do {} while (0)
#define TS0(ws, k) do {} while (0)
#define TS1(ws, k) do {} while (0)

struct NoHook { DEVI void operator()() const {} };
template <int NS, int NC, int NI, int R, int LAG, int XI, int X0 = 0, class Issue, class Hook, class Hook0 = NoHook>
DEVI void loader_loop(Issue&& issue_, Hook&& hook, Hook0&& hook0 = NoHook{}) {
  auto& issue = issue_;
#pragma unroll
  for (int k = 0; k < R; ++k) issue(k, k);
  hook0();
  WAITV((R - 1) * NI + X0);
  BAR();
#pragma unroll
  for (int k = R; k < NS; ++k) issue(k, k);
#pragma unroll
  for (int k = 1; k < LAG; ++k) { WAITV((NS - 1 - k) * NI); BAR(); }
  int slot = 0;
#pragma unroll 1
  for (int k = LAG; k <= NC - NS + LAG - 1; ++k) {
    WAITV((NS - LAG - 1) * NI);
    BAR();
    issue(k - LAG + NS, slot);
    slot = (slot + 1 == NS) ? 0 : slot + 1;
  }
  hook();
#pragma unroll
  for (int k = NC - NS + LAG; k < NC; ++k) {
    WAITV((NC - k - 1) * NI + XI);
    BAR();
  }
}

template <int NM, int ND, int NV>
DEVI void sched_region() {
  constexpr int DPER = (ND + NM - 3) / (NM - 2);
  constexpr int VPER = (NV + NM - 1) / NM;
#pragma unroll
  for (int i = 0; i < NM; ++i) {
    __builtin_amdgcn_sched_group_barrier(0x008, 1, 0);
    if (i * DPER < ND) {
      __builtin_amdgcn_sched_group_barrier(0x002, 1, 0);
      __builtin_amdgcn_sched_group_barrier(0x100, DPER, 0);
    }
    if (VPER > 0) __builtin_amdgcn_sched_group_barrier(0x002, VPER, 0);
    __builtin_amdgcn_sched_group_barrier(0x004, 1, 0);
  }
}

template <int NC, int NS, int SLOT, int NM, int ND, int NV, class LD, class MM, class PRE>
DEVI void consumer_loop(LAS const char* lds, LD&& ld_, MM&& mm_, PRE&& pre) {
  auto& ld = ld_; auto& mm = mm_;
  BAR();
  ld(IC<0>{}, lds, IC<0>{});
  SB0();
  pre();
  SB0();
  LAS const char* sb = lds;
#pragma unroll 1
  for (int c = 0; c < NC - 1; ++c) {
    ld(IC<1>{}, sb, IC<1>{});
    mm(IC<0>{});
    sb = (sb == lds + (NS - 1) * SLOT) ? lds : sb + SLOT;
    sched_region<NM, ND, NV>();
    SB0();
    LGKM0();
    BAR();
    SB0();
    ld(IC<0>{}, sb, IC<0>{});
    mm(IC<1>{});
    sched_region<NM, ND, NV>();
    SB0();
  }
  ld(IC<1>{}, sb, IC<1>{});
  mm(IC<0>{});
  sched_region<NM, ND, NV>();
  SB0();
  mm(IC<1>{});
}

template <int NC, int NS, int SLOT, int NM, int ND, int CM, class LD, class MM, class PRE, class MID, class LA>
DEVI void consumer_loop_full(LAS const char* lds, LD&& ld, MM&& mm, PRE&& pre, MID&& mid, LA&& la) {
  BAR();
  ld(IC<0>{}, lds);
  SB0();
  pre();
  SB0();
  LAS const char* s1 = lds + SLOT;
  auto body = [&]() {
    LAS const char* s2 = (s1 == lds + (NS - 1) * SLOT) ? lds : s1 + SLOT;
    la();
    BAR();
    SB0();
    ld(IC<1>{}, s1);
    mm(IC<0>{});
    s1 = (s2 == lds + (NS - 1) * SLOT) ? lds : s2 + SLOT;
    sched_region<NM, ND, 0>();
    SB0();
    BAR();
    SB0();
    ld(IC<0>{}, s2);
    mm(IC<1>{});
    sched_region<NM, ND, 0>();
    SB0();
  };
#pragma unroll 1
  for (int c = 0; c < CM; c += 2) body();
  mid();
  SB0();
#pragma unroll 1
  for (int c = CM; c + 2 < NC; c += 2) body();
  BAR();
  SB0();
  ld(IC<1>{}, s1);
  mm(IC<0>{});
  sched_region<NM, ND, 0>();
  SB0();
  mm(IC<1>{});
}

constexpr int K1_BM = 128, K1_BN = 160, K1_NS = 4;
constexpr int K1_AB = K1_BM * 128, K1_BB = 64 * K1_BN * 2, K1_SLOT = K1_AB + K1_BB;
constexpr int K1_TAB = K1_NS * K1_SLOT;
constexpr int K1_DUMMY = K1_TAB + 128 * 8 + 160 * 4;
constexpr int K1_LDS = K1_DUMMY + 256;

struct K1Args { const char* x; const char* wqkv; const char* w1; const char* w2; const char* w3; const char* w4; const char* wo; char* ws; const char* out; };

template <int MODE>
DEVI void k1_consumer(const K1Args& a, LAS char* lds, int wid, int lane, int panel, int ct, unsigned (&tc)[4], const char* pf0, const char* pf1) {
  constexpr bool KV = (MODE == 1);
  const int wr = wid >> 1, wc = wid & 1, g = lane >> 4, t = lane & 15;
  const int sw = t >> 1;
  unsigned aoff[2][2];
#pragma unroll
  for (int s = 0; s < 2; ++s)
#pragma unroll
    for (int hf = 0; hf < 2; ++hf) aoff[s][hf] = (wr * 64 + ((2 * hf + 2 * wc) & 3) * 16 + t) * 128 + (((4 * s + g) ^ sw) * 16);
  const int q = t >> 2, p = lane & 3;
  unsigned boff[5];
  if constexpr (KV) {
    const unsigned rowb = (8 * g + q) * 320 + 8 * p;
    const int odd = g & 1;
    const unsigned bev = K1_AB + rowb + 160 * wc + (odd ? ((wc & 1) ? -32 : 32) : 0);
    const unsigned bod = K1_AB + rowb + 160 * wc + (odd ? ((wc & 1) ? 32 : -32) : 0);
    boff[0] = wr ? bod + 96 : bev;
    boff[1] = wr ? bev + 128 : bod + 32;
    boff[2] = bev + 64;
    boff[3] = wr ? bev : bod + 96;
    boff[4] = wr ? bod + 32 : bev + 128;
  } else {
    const unsigned rowb = K1_AB + (8 * g + q) * 320 + 160 * wc;
    const int pp = p ^ (g & 1);
    const int pr0 = wr ? 64 : 0, pr1 = wr ? 0 : 64;
    const unsigned bE = rowb + pp * 16 + ((p & 1) ? 8 : 0), bO = rowb + pp * 16 + ((p & 1) ? 0 : 8);
    boff[0] = bE + pr0; boff[1] = bO + pr0; boff[2] = bE + pr1; boff[3] = bO + pr1;
    boff[4] = rowb + 128 + (((p >> 1) ^ (g & 1)) * 16) + (p & 1) * 8;
  }
  unsigned el[4][2];
  if constexpr (KV) {
#pragma unroll
    for (int i = 0; i < 4; ++i)
#pragma unroll
      for (int u = 0; u < 2; ++u) {
        int idx = (wid * 64 + lane + 256 * i) * 2 + u; if (idx >= 1680) idx = 0;
        int hl = idx / 420, rem = idx - hl * 420, w2 = hl >> 1, hb = hl & 1;
        int dd, k, col;
        if (rem < 400) {
          dd = rem / 20; int e = rem - dd * 20;
          int kc = 20 * hb + dd, vc = 40 + 20 * hb + e;
          int fa = kc >> 4, fb = vc >> 4;
          k = (fa == 0) ? fb - 2 : (fa == 1) ? fb : fb + 2;
          col = vc & 15; dd = kc & 15;
        } else {
          int kc = 20 * hb + (rem - 400);
          k = 7 + (kc >> 4); col = 0; dd = kc & 15;
        }
        el[i][u] = (((k * 64 + (dd >> 2) * 16 + col) * 4 + (dd & 3)) + (w2 & 1) * 2560) * 4;
        pin_v(el[i][u]);
      }
  }

  f32x4 acc[4][5];
#pragma unroll
  for (int i = 0; i < 4; ++i)
#pragma unroll
    for (int f = 0; f < 5; ++f) { acc[i][f] = f32x4{0.f, 0.f, 0.f, 0.f}; pin_v(acc[i][f]); }
  f32x4 s1[2], s2[2], cs[3];
#pragma unroll
  for (int j = 0; j < 2; ++j) { s1[j] = f32x4{0.f, 0.f, 0.f, 0.f}; s2[j] = s1[j]; pin_v(s1[j]); pin_v(s2[j]); }
#pragma unroll
  for (int j = 0; j < 3; ++j) { cs[j] = f32x4{0.f, 0.f, 0.f, 0.f}; pin_v(cs[j]); }
  const unsigned ONES = 0x3f803f80u;
  bf16x8 ones8 = __builtin_bit_cast(bf16x8, u32x4{ONES, ONES, ONES, ONES});
  pin_v(ones8);

  bf16x8 X[2][4], W[2][5];
  auto ld = [&](auto SET, LAS const char* sb, auto SS) {
    constexpr int st = decltype(SET)::value, s = decltype(SS)::value;
    if constexpr (KV) {
      X[st][0] = ld_frag(sb + aoff[s][0]);
#pragma unroll
      for (int f = 0; f < 5; ++f) { LAS const char* pb = sb + boff[f] + s * (32 * 320); W[st][f] = ld_tr(pb, pb + 4 * 320); }
      X[st][1] = ld_frag(sb + aoff[s][0] + 2048);
      X[st][2] = ld_frag(sb + aoff[s][1]);
      X[st][3] = ld_frag(sb + aoff[s][1] + 2048);
    } else {
      X[st][0] = ld_frag(sb + aoff[s][0]);
#pragma unroll
      for (int f = 0; f < 5; ++f) { LAS const char* pb = sb + boff[f] + s * (32 * 320); W[st][f] = ld_tr(pb, pb + 4 * 320); }
      X[st][1] = ld_frag(sb + aoff[s][0] + 2048);
      X[st][2] = ld_frag(sb + aoff[s][1]);
      X[st][3] = ld_frag(sb + aoff[s][1] + 2048);
    }
  };
  auto mm = [&](auto SET) {
    constexpr int st = decltype(SET)::value;
#pragma unroll
    for (int i = 0; i < 4; ++i)
#pragma unroll
      for (int f = 0; f < 5; ++f) acc[i][f] = KV ? mfma(X[st][i], W[st][f], acc[i][f]) : mfma(W[st][f], X[st][i], acc[i][f]);
#pragma unroll
    for (int j = 0; j < 2; ++j) { s1[j] = mfma(ones8, X[st][j], s1[j]); s2[j] = mfma(X[st][j], X[st][j], s2[j]); }
#pragma unroll
    for (int j = 0; j < 3; ++j) {
      if constexpr (KV) cs[j] = mfma(ones8, W[st][j], cs[j]);
      else cs[j] = mfma(W[st][j == 2 ? 4 : j], ones8, cs[j]);
    }
  };
  consumer_loop<10, K1_NS, K1_SLOT, 27, 14, 0>(lds, ld, mm, [&]() {
    TS1(a.ws, 0);
  });
  touch_done(tc);
  TS(a.ws, 0, 2);

  if constexpr (KV) {
    if (wr) {
#pragma unroll
      for (int i = 0; i < 4; ++i) {
        f32x4 u = acc[i][0]; acc[i][0] = acc[i][3]; acc[i][3] = u;
        u = acc[i][1]; acc[i][1] = acc[i][4]; acc[i][4] = u;
      }
    }
  }
  LAS float* mean = (LAS float*)(lds + K1_TAB);
  LAS float* rstd = mean + 128;
  LAS float* cst = mean + 256;
#pragma unroll
  for (int j = 0; j < 2; ++j) {
    const int r3 = t & 3;
    float a1 = s1[j][0], a2 = r3 == 0 ? s2[j][0] : r3 == 1 ? s2[j][1] : r3 == 2 ? s2[j][2] : s2[j][3];
    float m = a1 * (1.0f / 640.0f);
    float var = fmaxf(a2 * (1.0f / 640.0f) - m * m, 0.f);
    float r = __builtin_amdgcn_rsqf(var + 1e-5f);
    int row = wr * 64 + ((j + 2 * wc) & 3) * 16 + t;
    if (g == (t >> 2)) { mean[row] = m; rstd[row] = r; }
  }
#pragma unroll
  for (int j = 0; j < 3; ++j) {
    if constexpr (KV) {
      if (g == 0) cst[80 * wc + 16 * (j == 2 ? 2 : wr ? j + 3 : j) + t] = cs[j][0];
    } else {
      int col = (j == 2) ? 80 * wc + 64 + 4 * g : 80 * wc + (wr ? 32 : 0) + 8 * g + 4 * ((j ^ g) & 1);
      if (t == 0) *(LAS f32x4*)(cst + col) = cs[j];
    }
  }
  LGKM0();
  BAR();

  const rsrc_t rw = mk_rsrc(a.ws, WS_END);
  if constexpr (KV) {
    f32x4 M4[4], R4[4];
#pragma unroll
    for (int j = 0; j < 4; ++j) {
      int row = wr * 64 + ((j + 2 * wc) & 3) * 16 + 4 * g;
      M4[j] = *(LAS const f32x4*)(mean + row); R4[j] = *(LAS const f32x4*)(rstd + row);
    }
    bf16x8 op[2][5];
#pragma unroll
    for (int f = 0; f < 5; ++f) {
      const float c = cst[80 * wc + 16 * f + t];
      const bool isk = (f < 2) || (f == 2 && t < 8);
#pragma unroll
      for (int h = 0; h < 2; ++h) {
        u32x4 u;
#pragma unroll
        for (int jj = 0; jj < 2; ++jj) {
          const int j = 2 * h + jj;
          f32x2 y0 = lo2(R4[j]) * (lo2(acc[j][f]) - lo2(M4[j]) * c);
          f32x2 y1 = hi2(R4[j]) * (hi2(acc[j][f]) - hi2(M4[j]) * c);
          if (f <= 2) {
            f32x2 p0 = phi2(y0), p1 = phi2(y1);
            if (f < 2) { y0 = p0; y1 = p1; } else { y0 = isk ? p0 : y0; y1 = isk ? p1 : y1; }
          }
          u[2 * jj] = pack2v(y0); u[2 * jj + 1] = pack2v(y1);
        }
        op[h][f] = __builtin_bit_cast(bf16x8, u);
      }
    }
    const bf16x8 ones = __builtin_bit_cast(bf16x8, u32x4{ONES, ONES, ONES, ONES});
    f32x4 d[10];
    constexpr int pa[7] = {0, 0, 1, 1, 1, 2, 2}, pb[7] = {2, 3, 2, 3, 4, 3, 4};
#pragma unroll
    for (int k = 0; k < 7; ++k) { d[k] = mfma(op[0][pa[k]], op[0][pb[k]], f32x4{0.f, 0.f, 0.f, 0.f}); d[k] = mfma(op[1][pa[k]], op[1][pb[k]], d[k]); }
#pragma unroll
    for (int k = 0; k < 3; ++k) { d[7 + k] = mfma(op[0][k], ones, f32x4{0.f, 0.f, 0.f, 0.f}); d[7 + k] = mfma(op[1][k], ones, d[7 + k]); }
    LAS f32x4* red = (LAS f32x4*)lds;
#pragma unroll
    for (int k = 0; k < 10; ++k) red[(wid * 10 + k) * 64 + lane] = d[k];
    LGKM0();
    BAR();
    const unsigned kvp = WS_KVP + (panel * 32 + (ct - 4) * 4) * 420 * 2 + (wid * 64 + lane) * 4;
#pragma unroll
    for (int i = 0; i < 4; ++i) {
      float v0 = *(LAS const float*)(lds + el[i][0]) + *(LAS const float*)(lds + el[i][0] + 2 * 2560 * 4);
      float v1 = *(LAS const float*)(lds + el[i][1]) + *(LAS const float*)(lds + el[i][1] + 2 * 2560 * 4);
      if (i < 3 || wid * 64 + lane < 840 - 3 * 256) st4f<ST_AUX>(rw, kvp + i * 1024, __builtin_bit_cast(float, pack2(v0, v1)));
    }
  } else {
    constexpr bool isq = (MODE == 0);
    const int pr0 = wr ? 64 : 0, pr1 = wr ? 0 : 64;
    float m4[4], r4[4];
#pragma unroll
    for (int j = 0; j < 4; ++j) { int row = wr * 64 + ((j + 2 * wc) & 3) * 16 + t; m4[j] = mean[row]; r4[j] = rstd[row]; }
    if (ct == 12 && wc == 0 && g == 0) {
#pragma unroll
      for (int j = 0; j < 4; ++j) st8f<ST_AUX>(rw, WS_XST + (panel * 128 + wr * 64 + j * 16 + t) * 8, f32x2{m4[j], r4[j]});
    }
    f32x4 c4[5];
    {
      const int hE = 4 * (g & 1), hO = 4 - hE;
      c4[0] = *(LAS const f32x4*)(cst + 80 * wc + pr0 / 2 + 8 * g + hE);
      c4[1] = *(LAS const f32x4*)(cst + 80 * wc + pr0 / 2 + 8 * g + hO);
      c4[2] = *(LAS const f32x4*)(cst + 80 * wc + pr1 / 2 + 8 * g + hE);
      c4[3] = *(LAS const f32x4*)(cst + 80 * wc + pr1 / 2 + 8 * g + hO);
      c4[4] = *(LAS const f32x4*)(cst + 80 * wc + 64 + 4 * g);
    }
    const int blk = isq ? 2 * ct + wc : 2 * (ct - 12) + wc;
    const unsigned gs = isq ? 131072 : H_GS;
    const unsigned olo = (isq ? WS_PQ : WS_HPAD + 2 * 64) + 2 * blk * gs + 16 * g;
    const unsigned ohi = (isq ? WS_PQ + PQ_HI + blk * 65536 : WS_HHI + blk * H_GS2 + 4 * 32) + 8 * g;
    const bool od = g & 1;
#pragma unroll
    for (int j = 0; j < 4; ++j) {
      const int tok = panel * 128 + wr * 64 + ((j + 2 * wc) & 3) * 16 + t;
      const float m = m4[j];
      float r = r4[j];
      if (!isq) { float var = fmaxf(1.0f - 1e-5f * r * r, 0.f); r *= __builtin_amdgcn_rsqf(var + 1e-5f); }
      unsigned pk[5][2];
#pragma unroll
      for (int f = 0; f < 5; ++f) {
        f32x2 y0 = (lo2(acc[j][f]) - lo2(c4[f]) * m) * r, y1 = (hi2(acc[j][f]) - hi2(c4[f]) * m) * r;
        pk[f][0] = pack2v(isq ? phi2(y0) : gelu2(y0)); pk[f][1] = pack2v(isq ? phi2(y1) : gelu2(y1));
      }
      const unsigned o0 = olo + tok * 64 + (wr ? gs : 0), o1 = olo + tok * 64 + (wr ? 0 : gs), o2 = ohi + tok * 32;
      st16<ST_AUX>(rw, o0, od ? u32x4{pk[1][0], pk[1][1], pk[0][0], pk[0][1]} : u32x4{pk[0][0], pk[0][1], pk[1][0], pk[1][1]});
      st16<ST_AUX>(rw, o1, od ? u32x4{pk[3][0], pk[3][1], pk[2][0], pk[2][1]} : u32x4{pk[2][0], pk[2][1], pk[3][0], pk[3][1]});
      st8<ST_AUX>(rw, o2, u32x2{pk[4][0], pk[4][1]});
    }
    if (!isq && (panel == 0 || panel == 15) && wid == 0 && lane < 40)
      st16<ST_AUX>(rw, lane < 32 ? WS_HPAD + (4 * (ct - 12) + (lane >> 3)) * H_GS + (panel == 0 ? 0 : 2050) * 64 + (lane & 7) * 16
                                 : WS_HHI + (2 * (ct - 12) + ((lane - 32) >> 2)) * H_GS2 + (panel == 0 ? 2 : 2052) * 32 + (lane & 3) * 16, u32x4{0, 0, 0, 0});
  }
}

extern "C" __global__ __launch_bounds__(512) void k1_kernel(K1Args a) {
  extern __shared__ __attribute__((aligned(16))) char smem[];
  LAS char* lds = (LAS char*)smem;
  const int wid = __builtin_amdgcn_readfirstlane(threadIdx.x >> 6), lane = threadIdx.x & 63;
  const int xcd = blockIdx.x & 7, j = blockIdx.x >> 3;
  const int panel = (xcd & 1) * 8 + (j & 7), ct = (xcd >> 1) * 4 + (j >> 3);
  const bool kv = (ct >= 4 && ct < 12);
  TS0(a.ws, 0);
  if (wid >= 4) {
    const int w = wid - 4;
    unsigned voffA[4], voffB[5];
#pragma unroll
    for (int i = 0; i < 4; ++i) {
      int row = (w + 4 * i) * 8 + (lane >> 3), pc = (lane & 7) ^ ((row >> 1) & 7);
      voffA[i] = (panel * 128 + row) * 1280 + pc * 16;
    }
    const char* bB; unsigned ldb;
    if (ct < 4) { bB = a.wqkv + ct * 320; ldb = 3840; }
    else if (ct < 12) { bB = a.wqkv + 1280 + (ct - 4) * 160; ldb = 3840; }
    else { bB = a.w1 + (ct - 12) * 320; ldb = 1280; }
    const unsigned ksh = kv ? 1 : 0, kc1 = kv ? 1200 : 0, kc2 = kv ? 80 : 0;
#pragma unroll
    for (int i = 0; i < 5; ++i) {
      const unsigned P = (w + 4 * i) * 64 + lane, k = (P * 3277u) >> 16, qq = P - k * 20;
      const unsigned sp = qq ^ (((k >> 3) & 1) << ksh), blk = (sp * 52u) >> 8;
      voffB[i] = k * ldb + sp * 16 + (blk & 1) * kc1 - (blk >> 1) * kc2;
    }
    const int rot = ROT1 ? (((j & 7) * 5) / 4 + ((j >> 3) * 5) / 2) % 10 : 0;
    const char* bA = a.x;
    auto issue = [&](int k, int slot) {
      int c = k + rot; if (c >= 10) c -= 10;
      const char* pa = pin_s(bA + c * 128);
      const char* pb = pin_s(bB + (size_t)c * 64 * ldb);
      LAS char* sb = lds + slot * K1_SLOT + w * 1024;
#pragma unroll
      for (int i = 0; i < 4; ++i) { pin_v(voffA[i]); glds16(pa + voffA[i], sb + i * 4096); }
#pragma unroll
      for (int i = 0; i < 5; ++i) { pin_v(voffB[i]); glds16(pb + voffB[i], sb + K1_AB + i * 4096); }
    };
    loader_loop<K1_NS, 10, 9, 2, 1, (USE_PF ? 1 : 0) + (PFC ? 1 : 0) + (TLBW == 1 ? 1 : 0), PF1 == 3 ? 2 : 0>(issue, [&]() {
      {
        const char* cb = CODE_ADDR(k2_kernel);
        long len = CODE_ADDR(kz_end) - cb;
        long off = (long)(w * 64 + lane) * 128;
        if (off >= len) off = 0;
        __builtin_amdgcn_global_load_lds((GAS const void*)(cb + off), (LAS void*)(lds + K1_DUMMY), 4, 0, 0);
      }
      int id = blockIdx.x * 256 + w * 64 + lane;
      if (id >= 43520) id -= 43520;
      const char* p;
      if (id < 6400) p = a.w2 + (size_t)id * 128;
      else if (id < 12800) p = a.wo + (size_t)(id - 6400) * 128;
      else if (id < 28160) p = a.w3 + (size_t)(id - 12800) * 128;
      else p = a.w4 + (size_t)(id - 28160) * 128;
      __builtin_amdgcn_global_load_lds((GAS const void*)p, (LAS void*)(lds + K1_DUMMY), 4, 0, 0);
    }, [&]() {
    });
    return;
  }
  unsigned tc[4];
  tc[0] = prefetch_code(CODE_ADDR(k1_kernel), CODE_ADDR(k2_kernel), wid * 64 + lane);
  tc[1] = tc[0]; tc[2] = tc[0]; tc[3] = tc[0];
  const char *pf0 = a.x, *pf1 = a.x;
  {
    const char* p;
    const int tid = wid * 64 + lane, id = blockIdx.x * 144 + (tid < 144 ? tid : 143);
    if (id < 16384) p = a.x + (unsigned)((id >> 3) * 1280 + (2 + (id & 7)) * 128);
    else if (id < 31744) p = a.wqkv + (unsigned)(128 * 3840 + (id - 16384) * 128);
    else p = a.w1 + (unsigned)(128 * 1280 + (id - 31744) * 128);
    tc[1] = touch(p);
  }
  if (kv) k1_consumer<1>(a, lds, wid, lane, panel, ct, tc, pf0, pf1);
  else if (ct < 4) k1_consumer<0>(a, lds, wid, lane, panel, ct, tc, pf0, pf1);
  else k1_consumer<2>(a, lds, wid, lane, panel, ct, tc, pf0, pf1);
  TS(a.ws, 0, 3);
}

constexpr int T_NS = 7, T_AB = 96 * 128, T_BB = 64 * 128, T_SLOT = T_AB + T_BB;
constexpr int T_DUMMY = T_NS * T_SLOT;
constexpr int T_LDS = T_DUMMY + 256;

struct T96Ahead { const char* base; unsigned step, voff; };
template <int NC, int CM, int NB, class MID>
DEVI void t96_consumer(LAS char* lds, int wid, int lane, f32x4 (&acc)[3][2], char* ws, int kk, unsigned tc, MID&& mid, T96Ahead ah) {
  const int wr = wid >> 1, wc = wid & 1, g = lane >> 4, t = lane & 15, sw = t >> 1;
  unsigned aoff[2];
#pragma unroll
  for (int s = 0; s < 2; ++s) aoff[s] = (wr * 48 + t) * 128 + (((4 * s + g) ^ sw) * 16);
  const int q = t >> 2, p = lane & 3;
  const int swz = (((q >> 1) & 1) << 2) | (g & 1);
  const unsigned rowb = T_AB + (8 * g + q) * 128 + (((4 * wc + p) ^ swz) * 16);
  const unsigned bE = rowb + ((p & 1) ? 8 : 0), bO = rowb + ((p & 1) ? 0 : 8);
#pragma unroll
  for (int i = 0; i < 3; ++i) { acc[i][0] = f32x4{0.f, 0.f, 0.f, 0.f}; acc[i][1] = acc[i][0]; pin_v(acc[i][0]); pin_v(acc[i][1]); }
  bf16x8 X[2][2][3], W[2][2][2];
  auto ld = [&](auto SET, LAS const char* sb) {
    constexpr int st = decltype(SET)::value;
#pragma unroll
    for (int s = 0; s < 2; ++s) {
      X[st][s][0] = ld_frag(sb + aoff[s]);
      W[st][s][0] = ld_tr(sb + bE + s * 4096, sb + bE + s * 4096 + 512);
      W[st][s][1] = ld_tr(sb + bO + s * 4096, sb + bO + s * 4096 + 512);
      X[st][s][1] = ld_frag(sb + aoff[s] + 2048);
      X[st][s][2] = ld_frag(sb + aoff[s] + 4096);
    }
  };
  auto mm = [&](auto SET) {
    constexpr int st = decltype(SET)::value;
#pragma unroll
    for (int s = 0; s < 2; ++s)
#pragma unroll
      for (int i = 0; i < 3; ++i) { acc[i][0] = mfma(W[st][s][0], X[st][s][i], acc[i][0]); acc[i][1] = mfma(W[st][s][1], X[st][s][i], acc[i][1]); }
  };
  int nxt = T_NS + (wid >> 1);
  unsigned tl = 0;
  auto la = [&]() {
    if constexpr (NB > 0) {
      const int c = nxt < NC ? nxt : NC - 1;
      touch_s(tl, ah.voff, pin_s(ah.base + (size_t)c * ah.step));
      nxt += 2;
    }
  };
  auto pre = [&]() {
    TS1(ws, kk);
#pragma unroll
    for (int i = 0; i < NB; ++i) la();
  };
  consumer_loop_full<NC, T_NS, T_SLOT, 12, 14, CM>(lds, ld, mm, pre, mid, la);
  touch_done(tc); touch_done(tl);
  TS(ws, kk, 2);
}

template <int NC, bool SHIFT, bool GL>
DEVI void t96_loader(LAS char* lds, int w, int lane, const char* A, unsigned lda, int row0, const char* B, unsigned ldb, int rot) {
  unsigned voffA[3], voffB[2];
  const int pslot = lane & 7;
  int pcs[3];
#pragma unroll
  for (int i = 0; i < 3; ++i) {
    int row = (w + 4 * i) * 8 + (lane >> 3), pc = pslot ^ ((row >> 1) & 7);
    int gr = row0 + row; if (gr > S_ - 1) gr = S_ - 1;
    voffA[i] = GL ? (pc >> 2) * 131072 + gr * 64 + (pc & 3) * 16 : gr * lda + pc * 16; pcs[i] = pc * 8;
  }
#pragma unroll
  for (int i = 0; i < 2; ++i) {
    int k = (w + 4 * i) * 8 + (lane >> 3);
    int sp = pslot ^ ((((k >> 1) & 1) << 2) | ((k >> 3) & 1));
    voffB[i] = k * ldb + sp * 16;
  }
  auto issue = [&](int k, int slot) {
    int c = k + rot; if (c >= NC) c -= NC;
    const char* pa = pin_s(A + c * (GL ? 262144 : 128));
    const char* pb = pin_s(B + (size_t)c * 64 * ldb);
    LAS char* sb = lds + slot * T_SLOT + w * 1024;
#pragma unroll
    for (int i = 0; i < 3; ++i) {
      if constexpr (!SHIFT) pin_v(voffA[i]);
      unsigned vo = voffA[i];
      if constexpr (SHIFT) {
        int kc = c * 64 + pcs[i];
        vo += (kc < 160) ? LD_A : (kc < 320) ? 3 * LD_A : (kc < 480) ? 0 : 4 * LD_A;
      }
      if constexpr (SHIFT) pin_v(vo);
      glds16(pa + vo, sb + i * 4096);
    }
#pragma unroll
    for (int i = 0; i < 2; ++i) { pin_v(voffB[i]); glds16(pb + voffB[i], sb + T_AB + i * 4096); }
  };
  loader_loop<T_NS, NC, 5, T_R, 2, 0>(issue, []() {});
}

DEVI void k2_loader(LAS char* lds, int w, int lane, const char* ws, int row0, const char* B) {
  unsigned voffA[3], voffB[2], voffA2[3], voffB2[2];
  int dA[3];
  const int pslot = lane & 7;
#pragma unroll
  for (int i = 0; i < 3; ++i) {
    int row = (w + 4 * i) * 8 + (lane >> 3), pc = pslot ^ ((row >> 1) & 7);
    int gr = row0 + row; if (gr > S_ - 1) gr = S_ - 1;
    voffA[i] = (pc >> 2) * H_GS + gr * 64 + (pc & 3) * 16;
    voffA2[i] = (pc >> 1) * H_GS2 + (gr + (pc < 4 ? 3 : 5)) * 32 + (pc & 1) * 16;
    dA[i] = pc < 4 ? -32 : 32;
  }
#pragma unroll
  for (int i = 0; i < 2; ++i) {
    int k = (w + 4 * i) * 8 + (lane >> 3);
    int sp = pslot ^ ((((k >> 1) & 1) << 2) | ((k >> 3) & 1));
    voffB[i] = k * 1280 + sp * 16;
    voffB2[i] = (80 * (k >> 4) + 64 + (k & 15)) * 1280 + sp * 16;
  }
  auto issue = [&](int c, int slot) {
    const int sh = c < 2 ? 1 : c < 4 ? 3 : c < 6 ? 0 : 4;
    const char* pa = pin_s(c < 8 ? ws + WS_HPAD + c * (2 * H_GS) + sh * 64 : ws + WS_HHI + (c - 8) * (4 * H_GS2));
    const char* pb = pin_s(B + (c < 8 ? c * 80 : (c - 8) * 320) * 1280);
    LAS char* sb = lds + slot * T_SLOT + w * 1024;
    if (c == 8) {
#pragma unroll
      for (int i = 0; i < 3; ++i) voffA[i] = voffA2[i];
#pragma unroll
      for (int i = 0; i < 2; ++i) voffB[i] = voffB2[i];
    }
    if (c == 9) {
#pragma unroll
      for (int i = 0; i < 3; ++i) voffA[i] += dA[i];
    }
#pragma unroll
    for (int i = 0; i < 3; ++i) { pin_v(voffA[i]); glds16(pa + voffA[i], sb + i * 4096); }
#pragma unroll
    for (int i = 0; i < 2; ++i) { pin_v(voffB[i]); glds16(pb + voffB[i], sb + T_AB + i * 4096); }
  };
  loader_loop<T_NS, 10, 5, T_R, 2, 0>(issue, []() {});
}

DEVI void pair8(const f32x4& e, const f32x4& o, int g, f32x2 (&v)[4]) {
  const bool od = g & 1;
  v[0] = od ? lo2(o) : lo2(e); v[1] = od ? hi2(o) : hi2(e); v[2] = od ? lo2(e) : lo2(o); v[3] = od ? hi2(e) : hi2(o);
}
#define TMAP 1
DEVI bool t96_map(int xcd, int j, int& panel, int& ct, int& pin) {
  const int pg = xcd >> 1, np = pg < 2 ? 6 : 5, p0 = pg < 2 ? 6 * pg : 5 * pg + 2;
  pin = j / 5; ct = 5 * (xcd & 1) + j - pin * 5;
  panel = p0 + pin;
  return j < np * 5;
}

DEVI T96Ahead t96_ahead(const char* A, unsigned lda, int panel, const char* B, unsigned ldb, int xcd, int ct, int pin, int wid, int lane) {
  T96Ahead r;
  if (wid & 1) {
    const int np = xcd < 4 ? 6 : 5;
    int k = (lane < 13 ? lane : 12) * np + pin; if (k > 63) k = 63;
    r.base = B; r.step = 64 * ldb; r.voff = k * ldb;
  } else {
    int row = (lane < 20 ? lane : 19) * 5 + (ct >= 5 ? ct - 5 : ct); if (row > 95) row = 95;
    row += panel * 96; if (row > S_ - 1) row = S_ - 1;
    r.base = A; r.step = 128; r.voff = row * lda;
  }
  return r;
}

struct K2Args { const char* x; const char* w2; char* ws; };

DEVI void k2_side(const K2Args& a, LAS char* lds, int wid, int lane, int hg, int tq) {
  __builtin_amdgcn_s_waitcnt(0x0F70);
  const int tid = wid * 64 + lane, g = lane >> 4, t = lane & 15;
  u32x2 v[16];
  const int kt = tid < 420 ? tid : 0;
  {
    const rsrc_t rk = mk_rsrc(a.ws + WS_KVP, 16 * 32 * 420 * 2);
#pragma unroll
    for (int i = 0; i < 16; ++i) v[i] = __builtin_amdgcn_raw_buffer_load_b64(rk, (hg * 420 + kt) * 8, i * 32 * 840, 0);
  }
  *(LAS u32x4*)(lds + tid * 16) = u32x4{0, 0, 0, 0};
  if (tid < 16) *(LAS u32x4*)(lds + 8192 + tid * 16) = u32x4{0, 0, 0, 0};
  LGKM0();
  BAR();
  u32x4 B[4][4];
  {
    const rsrc_t rq = mk_rsrc(a.ws + WS_PQ, 2048 * 1280);
    const int tk0 = tq * 512 + wid * 64 + t, g2 = g < 2 ? g : 2;
    const unsigned lo0 = hg * 262144 + tk0 * 64, lo1 = lo0 + 131072;
    const unsigned b0 = lo0 + 16 * g, b1 = g < 2 ? lo0 + 40 + 16 * g : lo1, b2 = lo1 + 16 + 16 * g2;
    const unsigned b3 = g == 0 ? lo1 + 56 : PQ_HI + hg * 65536 + tk0 * 32 + 16 * (g2 - 1), s3 = g == 0 ? 1024 : 512;
#pragma unroll
    for (int i = 0; i < 4; ++i) {
      B[i][0] = ld16(rq, b0 + i * 1024); B[i][1] = ld16(rq, b1 + i * 1024); B[i][2] = ld16(rq, b2 + i * 1024); B[i][3] = ld16(rq, b3 + i * s3);
    }
  }
  SB0();
  {
    f32x2 sa[4], sb[4];
#pragma unroll
    for (int i = 0; i < 4; ++i) {
      sa[i] = (unpack2(v[i][0]) + unpack2(v[i + 4][0])) + (unpack2(v[i + 8][0]) + unpack2(v[i + 12][0]));
      sb[i] = (unpack2(v[i][1]) + unpack2(v[i + 4][1])) + (unpack2(v[i + 8][1]) + unpack2(v[i + 12][1]));
    }
    const f32x2 s0 = (sa[0] + sa[1]) + (sa[2] + sa[3]), s1 = (sb[0] + sb[1]) + (sb[2] + sb[3]);
    const int hd = kt / 105, r4 = (kt - hd * 105) * 4;
    unsigned p0 = pack2v(s0), p1 = pack2v(s1);
    pin_v(p0); pin_v(p1);
    if (tid < 420) {
      if (r4 < 400) {
        const int d0 = r4 / 20, e = r4 - d0 * 20;
        const int d = HLAY && ((hd == 1 && d0 >= 12) || (hd == 3 && d0 >= 4)) ? d0 + 4 : d0;
        const int row = e < 16 ? e : 4 * hd + e - 16;
        LAS char* q = lds + hd * 2048 + (e < 16 ? 0 : 1024) + ((d >> 3) * 16 + row) * 16 + (d & 7) * 2;
        *(LAS unsigned short*)(q) = (unsigned short)p0; *(LAS unsigned short*)(q + 16) = (unsigned short)(p0 >> 16);
        *(LAS unsigned short*)(q + 32) = (unsigned short)p1; *(LAS unsigned short*)(q + 48) = (unsigned short)(p1 >> 16);
      } else {
        const int d0 = r4 - 400;
        const int d = HLAY && ((hd == 1 && d0 >= 12) || (hd == 3 && d0 >= 4)) ? d0 + 4 : d0;
        *(LAS u32x2*)(lds + 8192 + hd * 64 + d * 2) = u32x2{p0, p1};
      }
    }
  }
  LGKM0();
  BAR();
  bf16x8 A[4][3];
#pragma unroll
  for (int j = 0; j < 4; ++j) {
    A[j][0] = ld_frag(lds + j * 2048 + lane * 16);
    A[j][1] = ld_frag(lds + j * 2048 + 1024 + lane * 16);
    A[j][2] = ld_frag(lds + 8192 + j * 64 + g * 16);
  }
  const rsrc_t rw = mk_rsrc(a.ws, WS_END);
#pragma unroll
  for (int i = 0; i < 4; ++i) {
    const f32x4 z4 = {0.f, 0.f, 0.f, 0.f};
    f32x4 d0[4], dz[4], d1 = z4;
#pragma unroll
    for (int j = 0; j < 4; ++j) {
      const bf16x8 b = __builtin_bit_cast(bf16x8, B[i][j]);
      dz[j] = mfma(A[j][2], b, z4); d0[j] = mfma(A[j][0], b, z4); d1 = mfma(A[j][1], b, d1);
    }
    float inv[4]; unsigned pk[4][2];
#pragma unroll
    for (int j = 0; j < 4; ++j) {
      inv[j] = __builtin_amdgcn_rcpf(dz[j][0] + 1e-6f);
      pk[j][0] = pack2v(lo2(d0[j]) * inv[j]); pk[j][1] = pack2v(hi2(d0[j]) * inv[j]);
    }
    const float ig = g == 0 ? inv[0] : g == 1 ? inv[1] : g == 2 ? inv[2] : inv[3];
    const unsigned orow = WS_O + hg * 262144 + (tq * 512 + wid * 64 + i * 16 + t) * 64 + 16 * g;
    st16<ST_AUX>(rw, orow, u32x4{pk[0][0], pk[0][1], pk[1][0], pk[1][1]});
    st16<ST_AUX>(rw, orow + 131072, u32x4{pk[2][0], pk[2][1], pk[3][0], pk[3][1]});
    st8<ST_AUX>(rw, WS_OHI + (hg * 2048 + tq * 512 + wid * 64 + i * 16 + t) * 32 + 8 * g, u32x2{pack2v(lo2(d1) * ig), pack2v(hi2(d1) * ig)});
  }
}
DEVI int o_row_lo(int k) { return 20 * (2 * (k >> 5) + ((k >> 2) & 1)) + 4 * ((k >> 3) & 3) + (k & 3); }
DEVI int o_row_hi(int k) { return 80 * (k >> 4) + 20 * ((k >> 2) & 3) + 16 + (k & 3); }

extern "C" __global__ __launch_bounds__(512) void k2_kernel(K2Args a) {
  extern __shared__ __attribute__((aligned(16))) char smem[];
  LAS char* lds = (LAS char*)smem;
  const int wid = __builtin_amdgcn_readfirstlane(threadIdx.x >> 6), lane = threadIdx.x & 63;
  const int xcd = blockIdx.x & 7, j0 = blockIdx.x >> 3;
  TS0(a.ws, 1);
  const int j = j0;
  {
    const int sj = xcd < 4 ? j - 30 : j - 25;
    if (sj >= 0) {
      if (sj >= 6) return;
      const int sidx = xcd < 4 ? xcd * 2 + sj : 8 + (xcd - 4) * 6 + sj;
      k2_side(a, lds, wid, lane, sidx >> 2, sidx & 3); TS(a.ws, 1, 3); return;
    }
  }
  int panel, ct, pin;
  if (!t96_map(xcd, j, panel, ct, pin)) return;
  if (wid >= 4) {
    k2_loader(lds, wid - 4, lane, a.ws, panel * 96, a.w2 + ct * 128);
    return;
    t96_loader<10, true, false>(lds, wid - 4, lane, a.ws + WS_HPAD, LD_A, panel * 96, a.w2 + ct * 128, 1280, USE_ROT ? (ct + (pin * 5) / 2) % 10 : 0);
    return;
  }
  const unsigned tc = prefetch_code(CODE_ADDR(k2_kernel), CODE_ADDR(k3_kernel), wid * 64 + lane);
  const int wr = wid >> 1, wc = wid & 1, g = lane >> 4, t = lane & 15;
  const rsrc_t rx = mk_rsrc(a.x, S_ * 1280), rw = mk_rsrc(a.ws, WS_END);
  u32x4 xv[3]; f32x2 st[3]; int tok[3];
  auto mid = [&]() {
#pragma unroll
    for (int i = 0; i < 3; ++i) {
      tok[i] = panel * 96 + wr * 48 + i * 16 + t;
      int tk = tok[i] < S_ ? tok[i] : S_ - 1;
      xv[i] = ld16(rx, tk * 1280 + ct * 128 + wc * 64 + g * 16);
      st[i] = ld8f(rw, WS_XST + tk * 8);
    }
  };
  f32x4 acc[3][2];
  t96_consumer<10, CM2, NB2>(lds, wid, lane, acc, a.ws, 1, tc, mid,
                             t96_ahead(a.ws + WS_HPAD + 2 * LD_A, LD_A, panel, a.w2 + ct * 128, 1280, xcd, ct, pin, wid, lane));
#pragma unroll
  for (int i = 0; i < 3; ++i) {
    f32x2 v[4];
    pair8(acc[i][0], acc[i][1], g, v);
    u32x4 o; float s1 = 0.f, s2 = 0.f;
#pragma unroll
    for (int e = 0; e < 4; ++e) {
      unsigned xn = pack2v((unpack2(xv[i][e]) - st[i][0]) * st[i][1]);
      o[e] = pack2v(unpack2(xn) + v[e]);
      s1 = dot2acc(o[e], 0x3f803f80u, s1); s2 = dot2acc(o[e], o[e], s2);
    }
    s1 = red_g(s1); s2 = red_g(s2);
    if (tok[i] < S_) {
      st16<ST_AUX>(rw, GLAY ? WS_X1 + (ct * 2 + wc) * 131072 + tok[i] * 64 + g * 16 : WS_X1 + tok[i] * LD_A + ct * 128 + wc * 64 + g * 16, o);
      if (g == 0) st8f<ST_AUX>(rw, WS_X1P + ((2 * ct + wc) * 2048 + tok[i]) * 8, f32x2{s1, s2});
    }
  }
  TS(a.ws, 1, 3);
}

constexpr int K3_NS = 4, K3_AB = 144 * 128, K3_BB = 64 * 256, K3_SLOT = K3_AB + K3_BB;
constexpr int K3_TAB = K3_NS * K3_SLOT;
constexpr int K3_DUMMY = K3_TAB + 144 * 8;
constexpr int K3_LDS = K3_DUMMY + 256;

struct K3Args { const char* w3; const char* wo; char* ws; };

template <bool MLP>
DEVI void k3_consumer(const K3Args& a, LAS char* lds, int wid, int lane, int panel, int ct, unsigned tc) {
  const int g = lane >> 4, t = lane & 15, sw = t >> 1;
  LAS f32x2* stat = (LAS f32x2*)(lds + K3_TAB);
  const rsrc_t rw = mk_rsrc(a.ws, WS_END);
  f32x2 part[20];
  const int tid = wid * 64 + lane;
  if constexpr (MLP) {
    int tk = panel * 144 + (tid < 144 ? tid : 0); if (tk > S_ - 1) tk = S_ - 1;
    if (wid < 3) {
#pragma unroll
      for (int i = 0; i < 20; ++i) part[i] = ld8f(rw, WS_X1P + (i * 2048 + tk) * 8);
    }
  }
  unsigned aoff[2];
#pragma unroll
  for (int s = 0; s < 2; ++s) aoff[s] = t * 128 + (((4 * s + g) ^ sw) * 16);
  const int q = t >> 2, p = lane & 3;
  const int swz = (q << 2) | (g & 1);
  const unsigned rowb = K3_AB + (8 * g + q) * 256 + (((4 * wid + p) ^ swz) * 16);
  const unsigned bE = rowb + ((p & 1) ? 8 : 0), bO = rowb + ((p & 1) ? 0 : 8);
  f32x4 acc[9][2];
#pragma unroll
  for (int i = 0; i < 9; ++i) { acc[i][0] = f32x4{0.f, 0.f, 0.f, 0.f}; acc[i][1] = acc[i][0]; pin_v(acc[i][0]); pin_v(acc[i][1]); }
  f32x4 cE = {0.f, 0.f, 0.f, 0.f}, cO = cE;
  bf16x8 ones8 = __builtin_bit_cast(bf16x8, u32x4{0x3f803f80u, 0x3f803f80u, 0x3f803f80u, 0x3f803f80u});
  if constexpr (MLP) { pin_v(cE); pin_v(cO); pin_v(ones8); }
  bf16x8 X[2][9], W[2][2];
  auto ld = [&](auto SET, LAS const char* sb, auto SS) {
    constexpr int st = decltype(SET)::value, s = decltype(SS)::value;
    X[st][0] = ld_frag(sb + aoff[s]);
    W[st][0] = ld_tr(sb + bE + s * 8192, sb + bE + s * 8192 + 1024);
    W[st][1] = ld_tr(sb + bO + s * 8192, sb + bO + s * 8192 + 1024);
#pragma unroll
    for (int j = 1; j < 9; ++j) X[st][j] = ld_frag(sb + aoff[s] + j * 2048);
  };
  auto mm = [&](auto SET) {
    constexpr int st = decltype(SET)::value;
#pragma unroll
    for (int i = 0; i < 9; ++i) { acc[i][0] = mfma(W[st][0], X[st][i], acc[i][0]); acc[i][1] = mfma(W[st][1], X[st][i], acc[i][1]); }
    if constexpr (MLP) { cE = mfma(W[st][0], ones8, cE); cO = mfma(W[st][1], ones8, cO); }
  };
  auto pre = [&]() {
    TS1(a.ws, 2);
    if constexpr (MLP) {
      if (wid < 3) {
#pragma unroll
        for (int i = 0; i < 10; ++i) part[i] += part[i + 10];
#pragma unroll
        for (int i = 0; i < 5; ++i) part[i] += part[i + 5];
        f32x2 s = (part[0] + part[1]) + (part[2] + part[3]) + part[4];
        float m = s[0] * (1.0f / 640.0f);
        float var = fmaxf(s[1] * (1.0f / 640.0f) - m * m, 0.f);
        if (tid < 144) stat[tid] = f32x2{m, __builtin_amdgcn_rsqf(var + 1e-5f)};
      }
    }
  };
  consumer_loop<10, K3_NS, K3_SLOT, MLP ? 20 : 18, 13, 0>(lds, ld, mm, pre);
  touch_done(tc);
  TS(a.ws, 2, 2);
  const rsrc_t ro = mk_rsrc(a.ws + (MLP ? WS_HID + (ct * 4 + wid) * 131072 : WS_S + ((ct - 12) * 4 + wid) * 131072), 131072);
  const unsigned rs = 64;
  const unsigned obase = g * 16 + (panel * 144 + t) * rs;
  const bool od = g & 1;
  f32x2 st[9];
  if constexpr (MLP) {
#pragma unroll
    for (int i = 0; i < 9; ++i) st[i] = stat[i * 16 + t];
  }
#pragma unroll
  for (int i = 0; i < 9; ++i) {
    f32x2 e0 = lo2(acc[i][0]), e1 = hi2(acc[i][0]), o0 = lo2(acc[i][1]), o1 = hi2(acc[i][1]);
    if constexpr (MLP) {
      e0 = gelu2((e0 - lo2(cE) * st[i][0]) * st[i][1]); e1 = gelu2((e1 - hi2(cE) * st[i][0]) * st[i][1]);
      o0 = gelu2((o0 - lo2(cO) * st[i][0]) * st[i][1]); o1 = gelu2((o1 - hi2(cO) * st[i][0]) * st[i][1]);
    }
    unsigned pe0 = pack2v(e0), pe1 = pack2v(e1), po0 = pack2v(o0), po1 = pack2v(o1);
    st16<ST_AUX>(ro, obase + i * 16 * rs, od ? u32x4{po0, po1, pe0, pe1} : u32x4{pe0, pe1, po0, po1});
  }
}

extern "C" __global__ __launch_bounds__(512) void k3_kernel(K3Args a) {
  extern __shared__ __attribute__((aligned(16))) char smem[];
  LAS char* lds = (LAS char*)smem;
  const int wid = __builtin_amdgcn_readfirstlane(threadIdx.x >> 6), lane = threadIdx.x & 63;
  const int xcd = blockIdx.x & 7, j = blockIdx.x >> 3;
  const int tile = xcd * 32 + j;
  if (tile >= 255) return;
  const int grp = tile / 68, rem = tile - grp * 68;
  int ct, pin;
  if (grp < 3) { ct = rem >> 2; pin = rem & 3; } else { ct = rem / 3; pin = rem - ct * 3; }
  const int panel = grp * 4 + pin;
  const bool mlp = ct < 12;
  TS0(a.ws, 2);
  if (wid >= 4) {
    const int w = wid - 4;
    const char* A = mlp ? a.ws + WS_X1 : a.ws + WS_O;
    const char* B = mlp ? a.w3 + ct * 256 : a.wo + (ct - 12) * 256;
    const unsigned ldb = mlp ? 3072 : 1280;
    unsigned voffA[5], voffA2[5], voffB[4], voffB2[4];
#pragma unroll
    for (int i = 0; i < 5; ++i) {
      int ii = w + 4 * i; if (ii > 17) ii = 17;
      int row = ii * 8 + (lane >> 3), pc = (lane & 7) ^ ((row >> 1) & 7);
      int gr = panel * 144 + row; if (gr > S_ - 1) gr = S_ - 1;
      voffA[i] = (mlp ? !GLAY : !OLAY) ? gr * LD_A + pc * 16 : (pc >> 2) * 131072 + gr * 64 + (pc & 3) * 16;
      voffA2[i] = mlp ? voffA[i] : (pc >> 1) * 65536 + gr * 32 + (pc & 1) * 16;
    }
#pragma unroll
    for (int i = 0; i < 4; ++i) {
      int P = (w + 4 * i) * 64 + lane, k = P >> 4, qq = P & 15;
      int sp = qq ^ (((k & 3) << 2) | ((k >> 3) & 1));
      voffB[i] = (mlp ? k : o_row_lo(k)) * ldb + sp * 16;
      voffB2[i] = (mlp ? k : o_row_hi(k)) * ldb + sp * 16;
    }
    const int last = (w + 16 > 17) ? 17 : w + 16;
    const int rot = USE_ROT ? ((pin * 5) / 2 + ((ct & 7) * 5) / 4) % 10 : 0;
    auto issue = [&](int k, int slot) {
      int c = k + rot; if (c >= 10) c -= 10;
      const char* pa = pin_s((mlp || c < 8) ? A + c * ((mlp ? !GLAY : !OLAY) ? 128 : 262144) : a.ws + WS_OHI + (c - 8) * 262144);
      const char* pb = pin_s(B + (size_t)(mlp ? c * 64 : c < 8 ? c * 80 : (c - 8) * 320) * ldb);
      LAS char* sb = lds + slot * K3_SLOT;
      if (c == 8) {
#pragma unroll
        for (int i = 0; i < 5; ++i) voffA[i] = voffA2[i];
#pragma unroll
        for (int i = 0; i < 4; ++i) voffB[i] = voffB2[i];
      }
#pragma unroll
      for (int i = 0; i < 4; ++i) { pin_v(voffA[i]); glds16(pa + voffA[i], sb + (w + 4 * i) * 1024); }
      pin_v(voffA[4]);
      glds16(pa + voffA[4], sb + last * 1024);
#pragma unroll
      for (int i = 0; i < 4; ++i) { pin_v(voffB[i]); glds16(pb + voffB[i], sb + K3_AB + (w + 4 * i) * 1024); }
    };
    loader_loop<K3_NS, 10, 9, 2, 1, 0>(issue, []() {});
    return;
  }
  const unsigned tc = prefetch_code(CODE_ADDR(k3_kernel), CODE_ADDR(k4_kernel), wid * 64 + lane);
  if (mlp) k3_consumer<true>(a, lds, wid, lane, panel, ct, tc);
  else k3_consumer<false>(a, lds, wid, lane, panel, ct, tc);
  TS(a.ws, 2, 3);
}

struct K4Args { const char* x; const char* w4; char* ws; char* out; };

extern "C" __global__ __launch_bounds__(512) void k4_kernel(K4Args a) {
  extern __shared__ __attribute__((aligned(16))) char smem[];
  LAS char* lds = (LAS char*)smem;
  const int wid = __builtin_amdgcn_readfirstlane(threadIdx.x >> 6), lane = threadIdx.x & 63;
  const int xcd = blockIdx.x & 7, j = blockIdx.x >> 3;
  int panel, ct, pin;
  if (!t96_map(xcd, j, panel, ct, pin)) return;
  TS0(a.ws, 3);
  if (wid >= 4) {
    t96_loader<24, false, GLAY>(lds, wid - 4, lane, a.ws + WS_HID, LD_HID, panel * 96, a.w4 + ct * 128, 1280, ROT4);
    return;
  }
  const unsigned tc = prefetch_code(CODE_ADDR(k4_kernel), CODE_ADDR(kz_end), wid * 64 + lane);
  const int wr = wid >> 1, wc = wid & 1, g = lane >> 4, t = lane & 15;
  const rsrc_t rx = mk_rsrc(a.x, S_ * 1280), rw = mk_rsrc(a.ws, WS_END), ro = mk_rsrc(a.out, S_ * 1280);
  u32x4 xv[3], x1v[3], sv[3]; int tok[3];
  auto mid = [&]() {
#pragma unroll
    for (int i = 0; i < 3; ++i) {
      tok[i] = panel * 96 + wr * 48 + i * 16 + t;
      int tk = tok[i] < S_ ? tok[i] : S_ - 1;
      unsigned off = tk * 1280 + ct * 128 + wc * 64 + g * 16;
      xv[i] = ld16(rx, off);
      const unsigned goff = (ct * 2 + wc) * 131072 + tk * 64 + g * 16;
      x1v[i] = ld16(rw, GLAY ? WS_X1 + goff : WS_X1 + tk * LD_A + ct * 128 + wc * 64 + g * 16);
      sv[i] = ld16(rw, WS_S + (GLAY ? goff : off));
    }
  };
  f32x4 acc[3][2];
  t96_consumer<24, CM4, NB4>(lds, wid, lane, acc, a.ws, 3, tc, mid,
                             t96_ahead(a.ws + WS_HID, LD_HID, panel, a.w4 + ct * 128, 1280, xcd, ct, pin, wid, lane));
#pragma unroll
  for (int i = 0; i < 3; ++i) {
    f32x2 v[4];
    pair8(acc[i][0], acc[i][1], g, v);
    u32x4 o;
#pragma unroll
    for (int e = 0; e < 4; ++e) o[e] = pack2v(unpack2(xv[i][e]) + unpack2(sv[i][e]) * silu2(unpack2(x1v[i][e]) + v[e]));
    st16<OUT_AUX>(ro, tok[i] * 1280 + ct * 128 + wc * 64 + g * 16, o);
  }
  TS(a.ws, 3, 3);
}

extern "C" __global__ void kz_end() {}

extern "C" void kernel_launch(void* const* d_in, const int* in_sizes, int n_in, void* d_out, int out_size, void* d_ws, size_t ws_size,
                              hipStream_t stream) {
  const char* x = (const char*)d_in[0];
  const char* wqkv = (const char*)d_in[1];
  const char* wo = (const char*)d_in[2];
  const char* w1 = (const char*)d_in[4];
  const char* w2 = (const char*)d_in[5];
  const char* w3 = (const char*)d_in[6];
  const char* w4 = (const char*)d_in[7];
  char* ws = (char*)d_ws;
  (void)hipFuncSetAttribute((const void*)k1_kernel, hipFuncAttributeMaxDynamicSharedMemorySize, K1_LDS);
  (void)hipFuncSetAttribute((const void*)k2_kernel, hipFuncAttributeMaxDynamicSharedMemorySize, T_LDS);
  (void)hipFuncSetAttribute((const void*)k3_kernel, hipFuncAttributeMaxDynamicSharedMemorySize, K3_LDS);
  (void)hipFuncSetAttribute((const void*)k4_kernel, hipFuncAttributeMaxDynamicSharedMemorySize, T_LDS + 96 * 128);
  k1_kernel<<<256, 512, K1_LDS, stream>>>(K1Args{x, wqkv, w1, w2, w3, w4, wo, ws, (const char*)d_out});
  k2_kernel<<<256, 512, T_LDS, stream>>>(K2Args{x, w2, ws});
  k3_kernel<<<256, 512, K3_LDS, stream>>>(K3Args{w3, wo, ws});
  k4_kernel<<<256, 512, T_LDS + 96 * 128, stream>>>(K4Args{x, w4, ws, (char*)d_out});
}
```
